```python
import torch

def _softmax(x, axis=-1):
    return torch.softmax(x, dim=axis)
def _gelu(x):
    return torch.nn.functional.gelu(x, approximate="tanh")
def _silu(x):
    return torch.nn.functional.silu(x)
def _sigmoid(x):
    return torch.sigmoid(x)
def _layernorm(x, axis=-1, eps=1e-5):
    m = x.mean(dim=axis, keepdim=True)
    v = x.var(dim=axis, keepdim=True, unbiased=False)
    return (x - m) / torch.sqrt(v + eps)
def _scatter_add(tgt, idx, src, axis=0):
    return tgt.clone().index_add_(axis, idx, src)
def _segment_sum(src, seg, n):
    out = torch.zeros((n,) + src.shape[1:], dtype=src.dtype)
    return out.index_add_(0, seg, src)
def _pad(x, pw):
    flat = tuple(v for pair in reversed(pw) for v in pair)
    return torch.nn.functional.pad(x, flat)

kernel_name = 'parallel_sum__grouped_kv_mqa__glu_gated_swiglu__prompt'

def setup_inputs():

    _w = (lambda a: (lambda t: t.to(torch.bfloat16) if t.is_floating_point() else t)(torch.as_tensor(a)))
    def _A_setup():
        B, S, D, H = 1, 2048, 576, 32
        Dh = D // H
        return {
            "x": _w((lambda *s: torch.randn(*s, dtype=torch.bfloat16) * 0.05)(B, S, D)),
            "wq": _w((lambda *s: torch.randn(*s, dtype=torch.bfloat16) * 0.05)(D, H * Dh)),
            "wk": _w((lambda *s: torch.randn(*s, dtype=torch.bfloat16) * 0.05)(D, Dh)),
            "wv": _w((lambda *s: torch.randn(*s, dtype=torch.bfloat16) * 0.05)(D, Dh)),
            "wo": _w((lambda *s: torch.randn(*s, dtype=torch.bfloat16) * 0.05)(H * Dh, D)),
        }
    _A_in = _A_setup()

    def _B_setup():
        B, S, D, F = 1, 2048, 576, 4608
        return {
            "x": _w((lambda *s: torch.randn(*s, dtype=torch.bfloat16) * 0.05)(B, S, D)),
            "w1": _w((lambda *s: torch.randn(*s, dtype=torch.bfloat16) * 0.4)(D, F) * (2.0 / D) ** 0.5),
            "w3": _w((lambda *s: torch.randn(*s, dtype=torch.bfloat16) * 0.4)(D, F) * (2.0 / D) ** 0.5),
            "w2": _w((lambda *s: torch.randn(*s, dtype=torch.bfloat16) * 0.4)(F, D) * (2.0 / F) ** 0.5),
        }
    _B_in = _B_setup()

    return {
        "A_x": _A_in["x"],
        "A_wq": _A_in["wq"],
        "A_wk": _A_in["wk"],
        "A_wv": _A_in["wv"],
        "A_wo": _A_in["wo"],
        "B_x": _B_in["x"],
        "B_w1": _B_in["w1"],
        "B_w3": _B_in["w3"],
        "B_w2": _B_in["w2"],
    }

def reference(A_x, A_wq, A_wk, A_wv, A_wo, B_x, B_w1, B_w3, B_w2):
    def _A_ref(x, wq, wk, wv, wo):
        B, S, D = x.shape
        H = 32
        Dh = D // H
        q = (lambda x, *d: x.permute(*d))((x @ wq).reshape(B, S, H, Dh), 0, 2, 1, 3)
        k = x @ wk
        v = x @ wv
        scores = torch.einsum("bhqd,bkd->bhqk", q, k) * (Dh ** -0.5)
        p = _softmax(scores, axis=-1)
        o = torch.einsum("bhqk,bkd->bhqd", p, v)
        return (lambda x, *d: x.permute(*d))(o, 0, 2, 1, 3).reshape(B, S, H * Dh) @ wo

    def _B_ref(x, w1, w3, w2):
        gate = torch.einsum("bsd,df->bsf", x, w1)
        up = torch.einsum("bsd,df->bsf", x, w3)
        h = _silu(gate) * up
        out = torch.einsum("bsf,fd->bsd", h, w2)
        return out

    x = A_x
    xn = _layernorm(x, axis=-1)
    return x + _A_ref(xn, A_wq, A_wk, A_wv, A_wo) + _B_ref(xn, B_w1, B_w3, B_w2)
```

```cpp
#include <hip/hip_runtime.h>
#include <hip/hip_bf16.h>
#include <stdint.h>

typedef __attribute__((ext_vector_type(8))) __bf16 bf16x8;
typedef __attribute__((ext_vector_type(2))) __bf16 bf16x2;
typedef __attribute__((ext_vector_type(4))) short s16x4;
typedef __attribute__((ext_vector_type(4))) float f32x4;
typedef __attribute__((ext_vector_type(16))) float f32x16;
typedef __attribute__((ext_vector_type(4))) unsigned u32x4;
typedef __attribute__((ext_vector_type(2))) unsigned u32x2;
typedef u32x4 __attribute__((aligned(4))) u32x4_a4;
typedef u32x2 __attribute__((aligned(4))) u32x2_a4;
#define LDS_AS __attribute__((address_space(3)))

constexpr int SEQ = 2048, DM = 576, FF = 4608, DH = 18;
constexpr size_t WS_H = 0;
constexpr size_t WS_Q = WS_H + 128ull * 144 * 1024;
constexpr size_t WS_KF = WS_Q + 2048ull * 576 * 2;
constexpr size_t WS_VF = WS_KF + 64 * 2048;
constexpr size_t WS_KMAX = WS_VF + 64 * 2048;
constexpr size_t WS_O = WS_KMAX + 256;
constexpr size_t WS_P = WS_O + 2048ull * 576 * 2;
constexpr size_t WS_WOF = WS_P + 4ull * 128 * 36 * 1024;

#define SBAR() __builtin_amdgcn_sched_barrier(0)
#define MFMA16(a, b, c) __builtin_amdgcn_mfma_f32_16x16x32_bf16(a, b, c, 0, 0, 0)
#define MFMA32(a, b, c) __builtin_amdgcn_mfma_f32_32x32x16_bf16(a, b, c, 0, 0, 0)

__device__ __forceinline__ void glds16(unsigned voff, const char* sbase, unsigned lds_base) {
    unsigned sv;
    asm volatile("s_mov_b32 %0, m0\n\ts_mov_b32 m0, %3\n\ts_nop 0\n\tglobal_load_lds_dwordx4 %1, %2 offset:0\n\ts_mov_b32 m0, %0"
                 : "=&s"(sv) : "v"(voff), "s"(sbase), "s"(lds_base) : "memory");
}
__device__ __forceinline__ void glds16_if(int cond, unsigned voff, const char* sbase, unsigned lds_base) {
    unsigned sv;
    asm volatile("s_cmp_gt_i32 %4, 0\n\ts_cbranch_scc0 1f\n\ts_mov_b32 %0, m0\n\ts_mov_b32 m0, %3\n\ts_nop 0\n\t"
                 "global_load_lds_dwordx4 %1, %2 offset:0\n\ts_mov_b32 m0, %0\n1:"
                 : "=&s"(sv) : "v"(voff), "s"(sbase), "s"(lds_base), "s"(cond) : "memory", "scc");
}
#define PIN(x) asm volatile("" : "+v"(x))
__device__ __forceinline__ bf16x8 lds_b128(unsigned a) { return *(const LDS_AS bf16x8*)(size_t)a; }
__device__ __forceinline__ bf16x8 lds_tr2(unsigned a0, unsigned a1) {
    s16x4 lo = __builtin_amdgcn_ds_read_tr16_b64_v4i16((LDS_AS s16x4*)(size_t)a0);
    s16x4 hi = __builtin_amdgcn_ds_read_tr16_b64_v4i16((LDS_AS s16x4*)(size_t)a1);
    typedef __attribute__((ext_vector_type(8))) short s16x8;
    s16x8 r = __builtin_shufflevector(lo, hi, 0, 1, 2, 3, 4, 5, 6, 7);
    return __builtin_bit_cast(bf16x8, r);
}
__device__ __forceinline__ unsigned pack2(float lo, float hi) {
    bf16x2 v = {(__bf16)lo, (__bf16)hi};
    return __builtin_bit_cast(unsigned, v);
}
__device__ __forceinline__ float bf16r(float v) { return (float)(__bf16)v; }
__device__ __forceinline__ float dot2(unsigned a, unsigned b, float c) {
    return __builtin_amdgcn_fdot2_f32_bf16(__builtin_bit_cast(bf16x2, a), __builtin_bit_cast(bf16x2, b), c, false);
}
__device__ __forceinline__ float fsum8(bf16x8 f, float acc) {
    u32x4 u = __builtin_bit_cast(u32x4, f);
#pragma unroll
    for (int i = 0; i < 4; ++i) acc = dot2(u[i], 0x3f803f80u, acc);
    return acc;
}
__device__ __forceinline__ float fsq8(bf16x8 f, float acc) {
    u32x4 u = __builtin_bit_cast(u32x4, f);
#pragma unroll
    for (int i = 0; i < 4; ++i) acc = dot2(u[i], u[i], acc);
    return acc;
}

constexpr int G1_XB = 32768, G1_WP = 20480, G1_STAGE = G1_XB + 2 * G1_WP;
constexpr int G1_LDS = 2 * G1_STAGE;
constexpr int G1_NT = DM / 64;

__global__ __launch_bounds__(512, 2) void k_gemm1(const char* __restrict__ x, const char* __restrict__ wq, const char* __restrict__ wk,
                                                  const char* __restrict__ wv, const char* __restrict__ w1, const char* __restrict__ w3,
                                                  char* __restrict__ ws) {
    extern __shared__ __attribute__((aligned(16))) char smem[];
    const unsigned lds0 = (unsigned)(size_t)(LDS_AS char*)smem;
    const int lane = threadIdx.x & 63, w = __builtin_amdgcn_readfirstlane(threadIdx.x >> 6);
    const int wm = w & 3, wn = w >> 2, par = (wm ^ wn) & 1;
    const int bid = blockIdx.x, nt = (bid & 7) * 4 + ((bid >> 3) & 3), mt = bid >> 5;
    const int g = lane >> 4, l15 = lane & 15;

    const char* wbase; int wstride, nvalid;
    if (nt <= 28) { wbase = (wn ? w3 : w1) + nt * 320; wstride = FF * 2; nvalid = (nt == 28) ? 16 : 20; }
    else if (nt <= 30) { wbase = wq + (nt - 29) * 640 + wn * 320; wstride = DM * 2; nvalid = (nt == 30 && wn) ? 12 : 20; }
    else { wbase = wn ? wv : wk; wstride = DH * 2; nvalid = 0; }
    wbase += 16 * wm * wstride;
    unsigned voffW[5];
#pragma unroll
    for (int p = 0; p < 5; ++p) {
        const int idx = 64 * p + lane, row = idx / 20, cp = idx - row * 20, c = cp ^ (((row >> 3) & 1) << 1);
        int cb = (c < nvalid) ? c * 16 : 0;
        if (nvalid == 0) cb = (c == 1) ? 16 : (c == 2) ? 20 : 0;
        voffW[p] = row * wstride + cb;
    }
    const unsigned voffX = (lane >> 3) * (DM * 2) + (((lane & 7) ^ ((4 * (w & 1) + (lane >> 4)) & 7)) << 4);
    const char* xbase = x + (size_t)(mt * 256 + 8 * w) * (DM * 2);
    const unsigned dstX = lds0 + w * 1024, dstW = lds0 + G1_XB + wn * G1_WP + wm * 5120;
    auto dma = [&](int on, int T, int piece) {
        const unsigned buf = (T & 1) ? G1_STAGE : 0;
        if (piece < 4) glds16_if(on, voffX, xbase + piece * (64 * DM * 2) + T * 128, dstX + buf + piece * 8192);
        else glds16_if(on, voffW[piece - 4], wbase + (size_t)T * 64 * wstride, dstW + buf + (piece - 4) * 1024);
    };
#pragma unroll
    for (int p = 0; p < 9; ++p) dma(1, 0, p);
#pragma unroll
    for (int p = 0; p < 9; ++p) dma(1, 1, p);

    unsigned xb = lds0 + (64 * wm + l15) * 128 + (((4 * par + g) ^ (l15 >> 1)) << 4);
    const unsigned wcommon = lds0 + par * 10240 + (8 * g + ((lane >> 2) & 3)) * 320 + 160 * wn + (lane & 3) * 8;
    const int sw = (wn ? -32 : 32) * (g & 1);
    unsigned wbE = wcommon + sw, wbO = wcommon - sw;
    const int dK = par ? -10240 : 10240;

    f32x4 acc[10][4];
#pragma unroll
    for (int i = 0; i < 10; ++i)
#pragma unroll
        for (int m = 0; m < 4; ++m) acc[i][m] = f32x4{0.f, 0.f, 0.f, 0.f};
    float cs[10], xsum[4], xsq[4];
#pragma unroll
    for (int i = 0; i < 10; ++i) cs[i] = 0.f;
#pragma unroll
    for (int m = 0; m < 4; ++m) { xsum[m] = 0.f; xsq[m] = 0.f; }
    bf16x8 wf[5], xs[2][4];
    auto rdW = [&](int u) {
        const int part = u / 5, ub = u % 5;
        const unsigned a = ((ub & 1) ? wbO : wbE) + G1_XB + part * G1_WP + 32 * ub;
        return lds_tr2(a, a + 1280);
    };

    asm volatile("s_waitcnt vmcnt(9)\n\ts_barrier" ::: "memory");
#pragma unroll
    for (int m = 0; m < 4; ++m) xs[0][m] = lds_b128(xb + m * 2048);
#pragma unroll
    for (int u = 0; u < 4; ++u) wf[u] = rdW(u);
    SBAR();

    for (int T = 0; T < G1_NT; ++T) {
        const int dStage = (T & 1) ? -G1_STAGE : G1_STAGE;
        const int more = G1_NT - 2 - T;
#pragma unroll
        for (int U = 0; U < 20; ++U) {
            const int ls = U / 10, u = U % 10;
            if (U == 6) { wbE += dK; wbO += dK; }
            if (U == 16) {
                asm volatile("s_waitcnt vmcnt(0) lgkmcnt(0)\n\ts_barrier" ::: "memory");
                wbE += dStage - dK; wbO += dStage - dK; xb += dStage;
#pragma unroll
                for (int m = 0; m < 4; ++m) xs[0][m] = lds_b128(xb + m * 2048);
            }
            wf[(U + 4) % 5] = rdW((U + 4) % 10);
            if (U >= 2 && U < 6) xs[1][U - 2] = lds_b128((xb ^ 64) + (U - 2) * 2048);
            if (U >= 16) { dma(more, T + 2, 2 * (U - 16)); dma(more, T + 2, 2 * (U - 16) + 1); if (U == 19) dma(more, T + 2, 8); }
#pragma unroll
            for (int m = 0; m < 4; ++m) acc[u][m] = MFMA16(wf[U % 5], xs[ls][m], acc[u][m]);
            if (U < 5 || U >= 15) { cs[u] = fsum8(wf[U % 5], cs[u]); PIN(cs[u]); }
            if (U == 5) { xsum[0] = fsum8(xs[0][0], xsum[0]); PIN(xsum[0]); }
            if (U == 6) { xsq[0] = fsq8(xs[0][0], xsq[0]); PIN(xsq[0]); }
            if (U == 7) { xsum[1] = fsum8(xs[0][1], xsum[1]); PIN(xsum[1]); }
            if (U == 8) { xsq[1] = fsq8(xs[0][1], xsq[1]); PIN(xsq[1]); }
            if (U == 10) { xsum[2] = fsum8(xs[1][2], xsum[2]); PIN(xsum[2]); }
            if (U == 11) { xsq[2] = fsq8(xs[1][2], xsq[2]); PIN(xsq[2]); }
            if (U == 12) { xsum[3] = fsum8(xs[1][3], xsum[3]); PIN(xsum[3]); }
            if (U == 13) { xsq[3] = fsq8(xs[1][3], xsq[3]); PIN(xsq[3]); }
            SBAR();
        }
    }

    LDS_AS float* st = (LDS_AS float*)(size_t)(lds0 + G1_STAGE);
    LDS_AS float* cl = st + 1024;
#pragma unroll
    for (int m = 0; m < 4; ++m) {
        float a = xsum[m], b = xsq[m];
        a += __shfl_xor(a, 16); b += __shfl_xor(b, 16);
        a += __shfl_xor(a, 32); b += __shfl_xor(b, 32);
        if (g == 0) { st[(wn * 256 + 64 * wm + 16 * m + l15) * 2] = a; st[(wn * 256 + 64 * wm + 16 * m + l15) * 2 + 1] = b; }
    }
    if (wm < 2) {
#pragma unroll
        for (int i = 0; i < 10; ++i) {
            float a = cs[i];
            a += __shfl_xor(a, 16); a += __shfl_xor(a, 32);
            if (g == 0) cl[((wm * 2 + wn) * 10 + i) * 16 + l15] = a;
        }
    }
    __syncthreads();
    float mean[4], rstd[4];
#pragma unroll
    for (int m = 0; m < 4; ++m) {
        const int t = 64 * wm + 16 * m + l15;
        const float s = st[t * 2] + st[(256 + t) * 2], q = st[t * 2 + 1] + st[(256 + t) * 2 + 1];
        const float mu = s * (1.f / DM);
        const float var = fmaxf(q * (1.f / DM) - mu * mu, 0.f);
        mean[m] = bf16r(mu);
        rstd[m] = 1.f / bf16r(sqrtf(bf16r(bf16r(var) + 1e-5f)));
    }
    auto colsum = [&](int i) {
        f32x4 a = *(const LDS_AS f32x4*)(cl + (wn * 10 + i) * 16 + 4 * g), b = *(const LDS_AS f32x4*)(cl + ((2 + wn) * 10 + i) * 16 + 4 * g);
        return a + b;
    };

    if (nt <= 28) {
        unsigned pk[4][5][2];
#pragma unroll
        for (int ub = 0; ub < 5; ++ub) {
            const f32x4 ca = colsum(ub), cb = colsum(5 + ub);
#pragma unroll
            for (int m = 0; m < 4; ++m) {
                const float er = -1.4426950408889634f * rstd[m], r2 = rstd[m] * rstd[m];
                float hv[4];
#pragma unroll
                for (int r = 0; r < 4; ++r) {
                    const float gg = fmaf(-mean[m], ca[r], acc[ub][m][r]), uu = fmaf(-mean[m], cb[r], acc[5 + ub][m][r]);
                    const float e = __builtin_amdgcn_exp2f(gg * er);
                    hv[r] = (gg * uu * r2) * __builtin_amdgcn_rcpf(1.f + e);
                }
                pk[m][ub][0] = pack2(hv[0], hv[1]); pk[m][ub][1] = pack2(hv[2], hv[3]);
            }
        }
#pragma unroll
        for (int m = 0; m < 4; ++m) {
            char* hp = ws + WS_H + ((size_t)(mt * 16 + 4 * wm + m) * 144 + 5 * nt) * 1024 + lane * 16;
            if (wn == 0) {
                *(u32x4*)(hp) = u32x4{pk[m][0][0], pk[m][0][1], pk[m][1][0], pk[m][1][1]};
                *(u32x4*)(hp + 1024) = u32x4{pk[m][2][0], pk[m][2][1], pk[m][3][0], pk[m][3][1]};
                *(u32x2*)(hp + 2048) = u32x2{pk[m][4][0], pk[m][4][1]};
            } else {
                *(u32x2*)(hp + 2048 + 8) = u32x2{pk[m][0][0], pk[m][0][1]};
                *(u32x4*)(hp + 3072) = u32x4{pk[m][1][0], pk[m][1][1], pk[m][2][0], pk[m][2][1]};
                if (nt < 28) *(u32x4*)(hp + 4096) = u32x4{pk[m][3][0], pk[m][3][1], pk[m][4][0], pk[m][4][1]};
            }
        }
    } else if (nt <= 30) {
        const float qs = 0.23570226039551584f * 1.4426950408889634f;
#pragma unroll
        for (int i = 0; i < 10; ++i) {
            const int col = (nt - 29) * 320 + (i / 5) * 160 + (5 * wn + (i % 5)) * 16;
            if (col < DM) {
                const f32x4 c = colsum(i);
#pragma unroll
                for (int m = 0; m < 4; ++m) {
                    const float s = rstd[m] * qs;
                    float v[4];
#pragma unroll
                    for (int r = 0; r < 4; ++r) v[r] = fmaf(-mean[m], c[r], acc[i][m][r]) * s;
                    const int t = mt * 256 + 64 * wm + 16 * m + l15;
                    *(u32x2*)(ws + WS_Q + ((size_t)t * DM + col + 4 * g) * 2) = u32x2{pack2(v[0], v[1]), pack2(v[2], v[3])};
                }
            }
        }
    } else if (wn == 0) {
        const f32x4 c0 = colsum(0), c1 = colsum(1), c5 = colsum(5), c6 = colsum(6);
        float kmax = 0.f;
#pragma unroll
        for (int m = 0; m < 4; ++m) {
            const int t = mt * 256 + 64 * wm + 16 * m + l15, tile = t >> 5, key = t & 31;
            float k0[4], k1[4], v0[4], v1[4];
#pragma unroll
            for (int r = 0; r < 4; ++r) {
                k0[r] = bf16r(fmaf(-mean[m], c0[r], acc[0][m][r]) * rstd[m]);
                k1[r] = bf16r(fmaf(-mean[m], c1[r], acc[1][m][r]) * rstd[m]);
                v0[r] = fmaf(-mean[m], c5[r], acc[5][m][r]) * rstd[m];
                v1[r] = fmaf(-mean[m], c6[r], acc[6][m][r]) * rstd[m];
            }
            float n2 = k0[0] * k0[0] + k0[1] * k0[1] + k0[2] * k0[2] + k0[3] * k0[3];
            if (g == 1) n2 += k1[2] * k1[2] + k1[3] * k1[3];
            n2 += __shfl_xor(n2, 16); n2 += __shfl_xor(n2, 32);
            kmax = fmaxf(kmax, n2);
            char* kp = ws + WS_KF + (size_t)tile * 2048 + ((g >> 1) * 32 + key) * 16 + (g & 1) * 8;
            *(u32x2*)(kp) = u32x2{pack2(k0[0], k0[1]), pack2(k0[2], k0[3])};
            u32x2 e2 = u32x2{0u, 0u};
            if (g == 0) e2[0] = 0x3f80u;
            if (g == 1) e2[1] = pack2(k1[2], k1[3]);
            *(u32x2*)(kp + 1024) = e2;
            const int u = t & 15;
            __bf16* vp = (__bf16*)(ws + WS_VF + (size_t)tile * 2048 + ((t >> 4) & 1) * 1024 + ((u >> 2) & 1) * 512) + 4 * (u >> 3) + (u & 3);
#pragma unroll
            for (int r = 0; r < 4; ++r) {
                const int d = 4 * g + r;
                vp[d * 8] = (__bf16)v0[r];
                const float s = (d == 0) ? 1.f : (d == 6 || d == 7) ? v1[r] : 0.f;
                vp[(16 + d) * 8] = (__bf16)s;
            }
        }
#pragma unroll
        for (int o = 1; o < 16; o <<= 1) kmax = fmaxf(kmax, __shfl_xor(kmax, o));
        if (lane == 0) ((float*)(ws + WS_KMAX))[mt * 4 + wm] = kmax;
    }
}

constexpr int G2_HB = 32768, G2_STAGE = G2_HB + 128 * 288;
constexpr int G2_EX = 2 * G2_STAGE;
constexpr int MID_LDS = G2_EX + 24576;
constexpr float SM_SAFE = 48.f;

template <bool ONLINE>
__device__ __forceinline__ void attn_loop(const char* kp, const char* vp, const bf16x8 (&q1)[4], const bf16x8 (&q2)[4], f32x16 (&o)[4], float (&mrow)[4]) {
    bf16x8 kf[2][2], vf[2][2];
    kf[0][0] = *(const bf16x8*)(kp); kf[0][1] = *(const bf16x8*)(kp + 1024);
    vf[0][0] = *(const bf16x8*)(vp); vf[0][1] = *(const bf16x8*)(vp + 1024);
    const f32x16 zero = {};
    int nbar = 1;
    for (int it2 = 0; it2 < 32; it2 += 2) {
#pragma unroll
        for (int h2 = 0; h2 < 2; ++h2) {
            const int it = it2 + h2;
            if (it == nbar) { __builtin_amdgcn_s_barrier(); nbar = (nbar < 25) ? nbar + 3 : 64; }
            const int nx = (it + 1 < 32) ? it + 1 : it;
            kf[h2 ^ 1][0] = *(const bf16x8*)(kp + nx * 2048); kf[h2 ^ 1][1] = *(const bf16x8*)(kp + nx * 2048 + 1024);
            vf[h2 ^ 1][0] = *(const bf16x8*)(vp + nx * 2048); vf[h2 ^ 1][1] = *(const bf16x8*)(vp + nx * 2048 + 1024);
            f32x16 s[2];
            s[0] = MFMA32(kf[h2][0], q1[0], zero); s[0] = MFMA32(kf[h2][1], q2[0], s[0]);
#pragma unroll
            for (int qb = 0; qb < 4; ++qb) {
                if (qb < 3) { s[(qb + 1) & 1] = MFMA32(kf[h2][0], q1[qb + 1], zero); s[(qb + 1) & 1] = MFMA32(kf[h2][1], q2[qb + 1], s[(qb + 1) & 1]); }
                f32x16& c = s[qb & 1];
                if (ONLINE) {
                    float mx = c[0];
#pragma unroll
                    for (int r = 1; r < 16; ++r) mx = fmaxf(mx, c[r]);
                    mx = fmaxf(mx, __shfl_xor(mx, 32));
                    const float mn = fmaxf(mrow[qb], mx), al = __builtin_amdgcn_exp2f(mrow[qb] - mn);
                    mrow[qb] = mn;
#pragma unroll
                    for (int r = 0; r < 16; ++r) { o[qb][r] *= al; c[r] -= mn; }
                }
                u32x4 p0, p1;
#pragma unroll
                for (int r = 0; r < 4; ++r) p0[r] = pack2(__builtin_amdgcn_exp2f(c[2 * r]), __builtin_amdgcn_exp2f(c[2 * r + 1]));
                o[qb] = MFMA32(vf[h2][0], __builtin_bit_cast(bf16x8, p0), o[qb]);
#pragma unroll
                for (int r = 0; r < 4; ++r) p1[r] = pack2(__builtin_amdgcn_exp2f(c[8 + 2 * r]), __builtin_amdgcn_exp2f(c[9 + 2 * r]));
                o[qb] = MFMA32(vf[h2][1], __builtin_bit_cast(bf16x8, p1), o[qb]);
            }
        }
    }
}

__global__ __launch_bounds__(512, 2) void k_mid(const char* __restrict__ w2, const char* __restrict__ wo, char* __restrict__ ws) {
    extern __shared__ __attribute__((aligned(16))) char smem[];
    const unsigned lds0 = (unsigned)(size_t)(LDS_AS char*)smem;
    const int lane = threadIdx.x & 63, w = __builtin_amdgcn_readfirstlane(threadIdx.x >> 6);
    const int bid = blockIdx.x;
    if (w < 4) {
        const int sp = bid & 3, mt = ((bid >> 2) & 1) * 8 + (bid >> 5), nt = (bid >> 3) & 3;
        const int g = lane >> 4;
        unsigned voffW[9];
#pragma unroll
        for (int p = 0; p < 9; ++p) { const int idx = 64 * p + lane, row = idx / 18; voffW[p] = row * (DM * 2) + (idx - row * 18) * 16; }
        const unsigned voffH = lane * 16;
        const char* hbase = ws + WS_H + ((size_t)(mt * 8 + 2 * w) * 144 + sp * 36) * 1024;
        const char* wbase = w2 + ((size_t)(sp * 1152 + 32 * w) * DM + nt * 144) * 2;
        auto dma = [&](int T) {
            const unsigned buf = lds0 + ((T & 1) ? G2_STAGE : 0);
#pragma unroll
            for (int j = 0; j < 8; ++j) glds16(voffH, hbase + ((j >> 2) * 144 + 4 * T + (j & 3)) * 1024, buf + (w * 8 + j) * 1024);
#pragma unroll
            for (int p = 0; p < 9; ++p) glds16(voffW[p], wbase + (size_t)T * 128 * DM * 2, buf + G2_HB + w * 9216 + p * 1024);
        };
        dma(0);
        f32x4 acc[9][2];
#pragma unroll
        for (int b = 0; b < 9; ++b) { acc[b][0] = f32x4{0.f, 0.f, 0.f, 0.f}; acc[b][1] = f32x4{0.f, 0.f, 0.f, 0.f}; }
        unsigned hb = lds0 + w * 8192 + lane * 16;
        unsigned wb = lds0 + G2_HB + (4 * g + ((lane >> 2) & 3)) * 288 + (lane & 3) * 8;
        for (int T = 0; T < 9; ++T) {
            asm volatile("s_waitcnt vmcnt(0) lgkmcnt(0)\n\ts_barrier" ::: "memory");
            if (T + 1 < 9) dma(T + 1);
#pragma unroll
            for (int k4 = 0; k4 < 4; ++k4) {
                const bf16x8 h0 = lds_b128(hb + k4 * 1024), h1 = lds_b128(hb + 4096 + k4 * 1024);
#pragma unroll
                for (int b = 0; b < 9; ++b) {
                    const bf16x8 wf = lds_tr2(wb + k4 * 9216 + 32 * b, wb + k4 * 9216 + 32 * b + 16 * 288);
                    acc[b][0] = MFMA16(wf, h0, acc[b][0]);
                    acc[b][1] = MFMA16(wf, h1, acc[b][1]);
                }
            }
            const int d = (T & 1) ? -G2_STAGE : G2_STAGE;
            hb += d; wb += d;
        }
#pragma unroll
        for (int m = 0; m < 2; ++m)
#pragma unroll
            for (int b = 0; b < 9; ++b)
                *(f32x4*)(ws + WS_P + (((size_t)sp * 128 + mt * 8 + 2 * w + m) * 36 + nt * 9 + b) * 1024 + lane * 16) = acc[b][m];
        const int fr = bid * 4 + w;
        if (fr < 36 * 18) {
            const int nb = fr / 18, ks = fr - nb * 18;
            const unsigned short* src = (const unsigned short*)wo + (size_t)(32 * ks + 8 * g) * DM + 16 * nb + (lane & 15);
            u32x4 v;
#pragma unroll
            for (int j = 0; j < 4; ++j) v[j] = (unsigned)src[(2 * j) * DM] | ((unsigned)src[(2 * j + 1) * DM] << 16);
            *(u32x4*)(ws + WS_WOF + (size_t)fr * 1024 + lane * 16) = v;
        }
    } else {
        __builtin_amdgcn_s_setprio(2);
        const int a = w - 4, qg = a & 1, kh = a >> 1, hi = lane >> 5;
        const int tok = (bid >> 3) * 64 + 32 * qg + (lane & 31), head0 = (bid & 7) * 4;
        float km = ((const float*)(ws + WS_KMAX))[lane & 31];
#pragma unroll
        for (int o = 1; o < 32; o <<= 1) km = fmaxf(km, __shfl_xor(km, o));
        bf16x8 q1[4], q2[4];
        float mrow[4];
        bool safe = true;
#pragma unroll
        for (int qb = 0; qb < 4; ++qb) {
            const char* qp = ws + WS_Q + ((size_t)tok * DM + (head0 + qb) * DH) * 2;
            const u32x4 a4 = *(const u32x4_a4*)(qp + 16 * hi);
            const unsigned e = hi ? 0u : *(const unsigned*)(qp + 32);
            float n2 = dot2(e, e, 0.f);
#pragma unroll
            for (int i = 0; i < 4; ++i) n2 = dot2(a4[i], a4[i], n2);
            n2 += __shfl_xor(n2, 32);
            const float bound = bf16r(sqrtf(n2 * km) * 1.01f);
            safe = safe && (bound <= SM_SAFE);
            mrow[qb] = bound;
            q1[qb] = __builtin_bit_cast(bf16x8, a4);
            q2[qb] = __builtin_bit_cast(bf16x8, u32x4{hi ? 0u : pack2(-bound, 0.f), 0u, 0u, e});
        }
        f32x16 o[4];
#pragma unroll
        for (int qb = 0; qb < 4; ++qb) o[qb] = f32x16{};
        const char* kp = ws + WS_KF + (size_t)kh * 32 * 2048 + lane * 16;
        const char* vp = ws + WS_VF + (size_t)kh * 32 * 2048 + lane * 16;
        if (__all(safe)) {
            attn_loop<false>(kp, vp, q1, q2, o, mrow);
        } else {
#pragma unroll
            for (int qb = 0; qb < 4; ++qb) {
                u32x4 t = __builtin_bit_cast(u32x4, q2[qb]); t[0] = 0u; q2[qb] = __builtin_bit_cast(bf16x8, t);
                mrow[qb] = -3.0e38f;
            }
            attn_loop<true>(kp, vp, q1, q2, o, mrow);
        }
        LDS_AS float* ex = (LDS_AS float*)(size_t)(lds0 + G2_EX);
        {
            LDS_AS float* dst = ex + a * 1536 + lane * 4;
#pragma unroll
            for (int j = 0; j < 2; ++j) {
                const f32x16& s = kh ? o[j] : o[2 + j];
                const float mm = kh ? mrow[j] : mrow[2 + j];
                *(LDS_AS f32x4*)(dst + j * 768) = f32x4{s[0], s[1], s[2], s[3]};
                *(LDS_AS f32x4*)(dst + j * 768 + 256) = f32x4{s[4], s[5], s[6], s[7]};
                *(LDS_AS f32x4*)(dst + j * 768 + 512) = f32x4{s[8], mm, s[10], s[11]};
            }
        }
        asm volatile("s_waitcnt lgkmcnt(0)\n\ts_barrier" ::: "memory");
        const LDS_AS float* src = ex + (a ^ 2) * 1536 + lane * 4;
#pragma unroll
        for (int j = 0; j < 2; ++j) {
            const f32x16& s = kh ? o[2 + j] : o[j];
            const float ma = kh ? mrow[2 + j] : mrow[j];
            const f32x4 t0 = *(const LDS_AS f32x4*)(src + j * 768), t1 = *(const LDS_AS f32x4*)(src + j * 768 + 256), t2 = *(const LDS_AS f32x4*)(src + j * 768 + 512);
            const float mb = t2[1], mm = fmaxf(ma, mb);
            const float fa = __builtin_amdgcn_exp2f(ma - mm), fb = __builtin_amdgcn_exp2f(mb - mm);
            float l = s[8] * fa + t2[0] * fb;
            l = __shfl(l, lane & 31);
            const float inv = 1.f / l, ia = fa * inv, ib = fb * inv;
            char* op = ws + WS_O + ((size_t)tok * DM + (head0 + 2 * kh + j) * DH) * 2;
            *(u32x2_a4*)(op + 8 * hi) = u32x2{pack2(s[0] * ia + t0[0] * ib, s[1] * ia + t0[1] * ib), pack2(s[2] * ia + t0[2] * ib, s[3] * ia + t0[3] * ib)};
            *(u32x2_a4*)(op + 16 + 8 * hi) = u32x2{pack2(s[4] * ia + t1[0] * ib, s[5] * ia + t1[1] * ib), pack2(s[6] * ia + t1[2] * ib, s[7] * ia + t1[3] * ib)};
            if (hi) *(unsigned*)(op + 32) = pack2(s[10] * ia + t2[2] * ib, s[11] * ia + t2[3] * ib);
        }
    }
}

constexpr int FIN_LDS = 4 * 18 * 1024;
__global__ __launch_bounds__(256, 1) void k_fin(const char* __restrict__ x, const char* __restrict__ ws, char* __restrict__ out) {
    extern __shared__ __attribute__((aligned(16))) char smem[];
    const unsigned lds0 = (unsigned)(size_t)(LDS_AS char*)smem;
    const int lane = threadIdx.x & 63, w = __builtin_amdgcn_readfirstlane(threadIdx.x >> 6);
    const int bid = blockIdx.x, tp = (bid & 7) * 8 + (bid >> 5), nt = (bid >> 3) & 3;
    const int g = lane >> 4, l15 = lane & 15;
    const int ks0 = (w * 9 + 1) >> 1;
    const bool five = !(w & 1);
    const char* op = ws + WS_O + ((size_t)(tp * 32 + l15) * DM + 32 * ks0 + 8 * g) * 2;
    const char* wp = ws + WS_WOF + ((size_t)nt * 9 * 18 + ks0) * 1024 + lane * 16;
    const char* pp = ws + WS_P + (((size_t)w * 128 + tp * 2) * 36 + nt * 9) * 1024 + lane * 16;
    bf16x8 of[5][2], wf[5][9];
    f32x4 acc[9][2];
#pragma unroll
    for (int k = 0; k < 5; ++k) {
        const int kk = (k < 4 || five) ? k : 0;
        of[k][0] = *(const bf16x8*)(op + kk * 64); of[k][1] = *(const bf16x8*)(op + 16 * DM * 2 + kk * 64);
#pragma unroll
        for (int b = 0; b < 9; ++b) wf[k][b] = *(const bf16x8*)(wp + (b * 18 + kk) * 1024);
    }
#pragma unroll
    for (int m = 0; m < 2; ++m)
#pragma unroll
        for (int b = 0; b < 9; ++b) acc[b][m] = *(const f32x4*)(pp + (m * 36 + b) * 1024);
    u32x2 xr[5];
#pragma unroll
    for (int j = 0; j < 5; ++j) {
        const int f = min(w + 4 * j, 17), m = f / 9, b = f - m * 9;
        xr[j] = *(const u32x2*)(x + ((size_t)(tp * 32 + 16 * m + l15) * DM + nt * 144 + 16 * b + 4 * g) * 2);
    }
    if (!five) { of[4][0] = bf16x8{}; of[4][1] = bf16x8{}; }
#pragma unroll
    for (int k = 0; k < 5; ++k)
#pragma unroll
        for (int b = 0; b < 9; ++b) { acc[b][0] = MFMA16(wf[k][b], of[k][0], acc[b][0]); acc[b][1] = MFMA16(wf[k][b], of[k][1], acc[b][1]); }
#pragma unroll
    for (int m = 0; m < 2; ++m)
#pragma unroll
        for (int b = 0; b < 9; ++b) *(LDS_AS f32x4*)(size_t)(lds0 + (w * 18 + m * 9 + b) * 1024 + lane * 16) = acc[b][m];
    __syncthreads();
#pragma unroll
    for (int j = 0; j < 5; ++j) {
        const int f = w + 4 * j;
        if (f < 18) {
            const int m = f / 9, b = f - m * 9;
            f32x4 s = *(const LDS_AS f32x4*)(size_t)(lds0 + f * 1024 + lane * 16);
#pragma unroll
            for (int v = 1; v < 4; ++v) s += *(const LDS_AS f32x4*)(size_t)(lds0 + (v * 18 + f) * 1024 + lane * 16);
            s[0] += __uint_as_float(xr[j][0] << 16); s[1] += __uint_as_float(xr[j][0] & 0xffff0000u);
            s[2] += __uint_as_float(xr[j][1] << 16); s[3] += __uint_as_float(xr[j][1] & 0xffff0000u);
            *(u32x2*)(out + ((size_t)(tp * 32 + 16 * m + l15) * DM + nt * 144 + 16 * b + 4 * g) * 2) = u32x2{pack2(s[0], s[1]), pack2(s[2], s[3])};
        }
    }
}

extern "C" void kernel_launch(void* const* d_in, const int* in_sizes, int n_in, void* d_out, int out_size, void* d_ws, size_t ws_size,
                              hipStream_t stream) {
    const char* x = (const char*)d_in[0];
    const char* wq = (const char*)d_in[1];
    const char* wk = (const char*)d_in[2];
    const char* wv = (const char*)d_in[3];
    const char* wo = (const char*)d_in[4];
    const char* w1 = (const char*)d_in[6];
    const char* w3 = (const char*)d_in[7];
    const char* w2 = (const char*)d_in[8];
    char* ws = (char*)d_ws;
    (void)hipFuncSetAttribute((const void*)k_gemm1, hipFuncAttributeMaxDynamicSharedMemorySize, G1_LDS);
    (void)hipFuncSetAttribute((const void*)k_mid, hipFuncAttributeMaxDynamicSharedMemorySize, MID_LDS);
    (void)hipFuncSetAttribute((const void*)k_fin, hipFuncAttributeMaxDynamicSharedMemorySize, FIN_LDS);
    k_gemm1<<<256, 512, G1_LDS, stream>>>(x, wq, wk, wv, w1, w3, ws);
    k_mid<<<256, 512, MID_LDS, stream>>>(w2, wo, ws);
    k_fin<<<256, 256, FIN_LDS, stream>>>(x, ws, (char*)d_out);
}
```

```cpp
#include <hip/hip_runtime.h>
#include <stdint.h>

typedef __attribute__((ext_vector_type(8))) __bf16 bf16x8;
typedef __attribute__((ext_vector_type(2))) __bf16 bf16x2;
typedef __attribute__((ext_vector_type(4))) short s16x4;
typedef __attribute__((ext_vector_type(8))) short s16x8;
typedef __attribute__((ext_vector_type(4))) float f32x4;
typedef __attribute__((ext_vector_type(2))) float f32x2;
typedef __attribute__((ext_vector_type(16))) float f32x16;
typedef __attribute__((ext_vector_type(4))) unsigned u32x4;
typedef __attribute__((ext_vector_type(2))) unsigned u32x2;
typedef unsigned long long u64;
#define LDS_AS __attribute__((address_space(3)))
#define GLB_AS __attribute__((address_space(1)))

constexpr int DM = 576, FF = 4608;
constexpr unsigned WS_EPOCH = 0, WS_F1 = 2048, WS_F2 = 4096, WS_F3 = 6144, WS_KMAX = 8192;
constexpr unsigned WS_H = 16384;
constexpr unsigned WS_Q1 = WS_H + 64u * 288 * 1024;
constexpr unsigned WS_Q2 = WS_Q1 + 64u * 32 * 1024;
constexpr unsigned WS_K1 = WS_Q2 + 64u * 8 * 512;
constexpr unsigned WS_K2 = WS_K1 + 2u * 64 * 1024;
constexpr unsigned WS_V = WS_K2 + 2u * 64 * 256;
constexpr unsigned WS_O = WS_V + 64u * 2048;
constexpr unsigned WS_P = WS_O + 64u * 36 * 1024;
constexpr unsigned WS_END = WS_P + 1024u * 18432;

constexpr int P1_XB = 32768, P1_WP = 20512, P1_STAGE = 73984;
constexpr int L_CNT = 2 * P1_STAGE;
constexpr int L_ST = L_CNT + 64;
constexpr int L_CS = L_ST + 2048;
constexpr int G_STAGE = 128 * 288;
constexpr int L_EX = 3 * G_STAGE;
constexpr int L_VT = L_EX + 24576;
constexpr int LDS_BYTES = 163840;
static_assert(L_CS + 10240 <= LDS_BYTES && L_VT + 8192 <= L_CNT, "lds map");

constexpr float SM_SAFE = 48.f;
constexpr unsigned SPIN_MAX = 1u << 21;

#define SBAR() __builtin_amdgcn_sched_barrier(0)
#define MFMA32(a, b, c) __builtin_amdgcn_mfma_f32_32x32x16_bf16(a, b, c, 0, 0, 0)
#define MFMA8(a, b, c) __builtin_amdgcn_mfma_f32_32x32x8bf16_1k(a, b, c, 0, 0, 0)
#define PIN(x) asm volatile("" : "+v"(x))
#define SC1 16

template <int N> struct IC { static constexpr int v = N; };
template <int I, int N, class F> __device__ __forceinline__ void static_for(F&& f) {
    if constexpr (I < N) { f(IC<I>{}); static_for<I + 1, N>(f); }
}

__device__ __forceinline__ void glds16(unsigned voff, const char* sbase, unsigned lds_base) {
    unsigned sv;
    asm volatile("s_mov_b32 %0, m0\n\ts_mov_b32 m0, %3\n\ts_nop 0\n\tglobal_load_lds_dwordx4 %1, %2 offset:0\n\ts_mov_b32 m0, %0"
                 : "=&s"(sv) : "v"(voff), "s"(sbase), "s"(lds_base) : "memory");
}
__device__ __forceinline__ void glds16_if(int cond, unsigned voff, const char* sbase, unsigned lds_base) {
    unsigned sv;
    asm volatile("s_cmp_gt_i32 %4, 0\n\ts_cbranch_scc0 1f\n\ts_mov_b32 %0, m0\n\ts_mov_b32 m0, %3\n\ts_nop 0\n\t"
                 "global_load_lds_dwordx4 %1, %2 offset:0\n\ts_mov_b32 m0, %0\n1:"
                 : "=&s"(sv) : "v"(voff), "s"(sbase), "s"(lds_base), "s"(cond) : "memory", "scc");
}
template <int OFF> __device__ __forceinline__ void ld16(bf16x8& d, unsigned voff, const char* sbase) {
    asm volatile("global_load_dwordx4 %0, %1, %2 offset:%3 sc1" : "=v"(d) : "v"(voff), "s"(sbase), "n"(OFF) : "memory");
}
template <int OFF> __device__ __forceinline__ void ld8(s16x4& d, unsigned voff, const char* sbase) {
    asm volatile("global_load_dwordx2 %0, %1, %2 offset:%3 sc1" : "=v"(d) : "v"(voff), "s"(sbase), "n"(OFF) : "memory");
}
template <int N, class T> __device__ __forceinline__ void waitv(T& a) { asm volatile("s_waitcnt vmcnt(%1)" : "+v"(a) : "n"(N) : "memory"); }
template <int N, class T, class U> __device__ __forceinline__ void waitv(T& a, U& b) { asm volatile("s_waitcnt vmcnt(%2)" : "+v"(a), "+v"(b) : "n"(N) : "memory"); }

__device__ __forceinline__ void st16(u32x4 v, __amdgpu_buffer_rsrc_t rs, unsigned voff, unsigned soff) {
    __builtin_amdgcn_raw_buffer_store_b128(v, rs, voff, soff, SC1);
    asm volatile("s_nop 1" :: "v"(v) : "memory");
}
__device__ __forceinline__ bf16x8 lds_b128(unsigned a) { return *(const LDS_AS bf16x8*)(size_t)a; }
__device__ __forceinline__ bf16x8 lds_tr2(unsigned a0, unsigned a1) {
    s16x4 lo = __builtin_amdgcn_ds_read_tr16_b64_v4i16((LDS_AS s16x4*)(size_t)a0);
    s16x4 hi = __builtin_amdgcn_ds_read_tr16_b64_v4i16((LDS_AS s16x4*)(size_t)a1);
    s16x8 r = __builtin_shufflevector(lo, hi, 0, 1, 2, 3, 4, 5, 6, 7);
    return __builtin_bit_cast(bf16x8, r);
}
__device__ __forceinline__ unsigned pack2(float lo, float hi) {
    bf16x2 v = {(__bf16)lo, (__bf16)hi};
    return __builtin_bit_cast(unsigned, v);
}
__device__ __forceinline__ float bf16r(float v) { return (float)(__bf16)v; }
__device__ __forceinline__ float blo(unsigned u) { return __uint_as_float(u << 16); }
__device__ __forceinline__ float bhi(unsigned u) { return __uint_as_float(u & 0xffff0000u); }
__device__ __forceinline__ float sum32(float a) {
    auto r = __builtin_amdgcn_permlane32_swap(__float_as_uint(a), __float_as_uint(a), false, false);
    return __uint_as_float(r[0]) + __uint_as_float(r[1]);
}
__device__ __forceinline__ float max32(float a) {
    auto r = __builtin_amdgcn_permlane32_swap(__float_as_uint(a), __float_as_uint(a), false, false);
    return fmaxf(__uint_as_float(r[0]), __uint_as_float(r[1]));
}
__device__ __forceinline__ float low32(float a) {
    auto r = __builtin_amdgcn_permlane32_swap(__float_as_uint(a), __float_as_uint(a), false, false);
    return __uint_as_float(r[0]);
}
__device__ __forceinline__ unsigned cnt_add(unsigned addr) {
    unsigned old, r;
    asm volatile("s_mov_b64 exec, 1\n\tds_add_rtn_u32 %0, %2, %3\n\ts_waitcnt lgkmcnt(0)\n\tv_readfirstlane_b32 %1, %0\n\ts_mov_b64 exec, -1"
                 : "=&v"(old), "=s"(r) : "v"(addr), "v"(1u) : "memory");
    return r;
}
__device__ __forceinline__ void cnt_post(unsigned addr) {
    asm volatile("s_mov_b64 exec, 1\n\tds_add_u32 %0, %1\n\ts_mov_b64 exec, -1" :: "v"(addr), "v"(1u) : "memory");
}
__device__ __forceinline__ void cnt_wait(unsigned addr, unsigned target) {
    unsigned v, t, lim = SPIN_MAX * 8;
    asm volatile("1:\n\tds_read_b32 %0, %3\n\ts_waitcnt lgkmcnt(0)\n\tv_readfirstlane_b32 %1, %0\n\ts_cmp_ge_u32 %1, %4\n\ts_cbranch_scc1 2f\n\t"
                 "s_sleep 1\n\ts_sub_u32 %2, %2, 1\n\ts_cbranch_scc0 1b\n2:"
                 : "=&v"(v), "=&s"(t), "+s"(lim) : "v"(addr), "s"(target) : "memory", "scc");
}
__device__ __forceinline__ void flag_set(char* ws, unsigned off, u64 tag, int lane) {
    if (lane == 0) __hip_atomic_store((GLB_AS u64*)(ws + off), tag, __ATOMIC_RELAXED, __HIP_MEMORY_SCOPE_AGENT);
}
__device__ __forceinline__ void flag_wait(char* ws, unsigned off, u64 tag) {
    u64 v; unsigned lim = SPIN_MAX;
    asm volatile("1:\n\tglobal_load_dwordx2 %0, %2, %3 sc1\n\ts_waitcnt vmcnt(0)\n\tv_cmp_ne_u64 vcc, %4, %0\n\ts_cmp_eq_u64 vcc, 0\n\ts_cbranch_scc1 2f\n\t"
                 "s_sleep 2\n\ts_sub_u32 %1, %1, 1\n\ts_cbranch_scc0 1b\n2:"
                 : "=&v"(v), "+s"(lim) : "v"(off), "s"(ws), "s"(tag) : "memory", "scc", "vcc");
}
__device__ __forceinline__ void row_acc(unsigned u, float& s, float& q0, float& q1) {
    float a, b;
    asm("v_lshlrev_b32 %3, 16, %5\n\tv_and_b32 %4, 0xffff0000, %5\n\tv_fma_f32 %1, %3, %3, %1\n\tv_fma_f32 %2, %4, %4, %2\n\t"
        "v_add_f32 %3, %3, %4\n\tv_add_f32 %0, %0, %3" : "+v"(s), "+v"(q0), "+v"(q1), "=&v"(a), "=&v"(b) : "v"(u));
}
__device__ __forceinline__ void col_acc(unsigned u0, unsigned u1, float& s) {
    float a, b, c, d;
    asm("v_lshlrev_b32 %1, 16, %5\n\tv_and_b32 %2, 0xffff0000, %5\n\tv_lshlrev_b32 %3, 16, %6\n\tv_and_b32 %4, 0xffff0000, %6\n\t"
        "v_add_f32 %1, %1, %2\n\tv_add_f32 %3, %3, %4\n\tv_add_f32 %0, %0, %1\n\tv_add_f32 %0, %0, %3"
        : "+v"(s), "=&v"(a), "=&v"(b), "=&v"(c), "=&v"(d) : "v"(u0), "v"(u1));
}

__device__ __forceinline__ int kperm(int r) { return (r & 8) + ((r & 1) << 2) + ((r >> 1) & 3); }

constexpr int g2_dma_at(int jj) { const int T = jj >> 3, j = jj & 7; return (T >= 1 && T + 2 < 9) ? (j == 0 ? 2 : 1) : 0; }
constexpr int g2_ops_at(int jj) { return g2_dma_at(jj) + (jj + 24 < 72 ? 1 : 0); }
constexpr int g2_after_h(int J) {
    int n = 0;
    if (J < 24) { n = 23 - J; for (int s = 0; s < J; ++s) n += g2_ops_at(s); }
    else for (int s = J - 23; s < J; ++s) n += g2_ops_at(s);
    return n;
}
constexpr int g2_after_dma(int T) {
    int n = (8 * (T - 2) + 7 + 24 < 72) ? 1 : 0;
    for (int s = 8 * (T - 1); s < 8 * T - 1; ++s) n += g2_ops_at(s);
    return n;
}

__global__ __launch_bounds__(512, 2) void k_all(const char* __restrict__ x, const char* __restrict__ wq, const char* __restrict__ wk,
                                                const char* __restrict__ wv, const char* __restrict__ wo, const char* __restrict__ w1,
                                                const char* __restrict__ w3, const char* __restrict__ w2, char* ws, char* __restrict__ out) {
    extern __shared__ __attribute__((aligned(16))) char smem[];
    const unsigned lds0 = (unsigned)(size_t)(LDS_AS char*)smem;
    const int lane = threadIdx.x & 63, w = __builtin_amdgcn_readfirstlane(threadIdx.x >> 6);
    const int bid = blockIdx.x;
    const int l31 = lane & 31, hi = lane >> 5;
    const __amdgpu_buffer_rsrc_t rs = __builtin_amdgcn_make_buffer_rsrc(ws, 0, WS_END, 0x00020000);
    const int sp = bid & 3, mt2 = ((bid >> 2) & 1) * 8 + (bid >> 5), nt2 = (bid >> 3) & 3;
    const unsigned trl = (2 * ((lane >> 2) & 3) + hi) * 288 + ((lane >> 4) & 1) * 32 + (lane & 3) * 8;
    u64 tag;
    asm volatile("global_load_dwordx2 %0, %1, %2 sc1" : "=v"(tag) : "v"(WS_EPOCH), "s"(ws) : "memory");
    {
        const int wm = w & 3, wn = w >> 2;
        const int nt = (bid & 7) * 4 + ((bid >> 3) & 3), mt = bid >> 5;
        const char* wbase; int wstride;
        if (nt <= 28) { wbase = (wn ? w3 : w1) + nt * 320; wstride = FF * 2; }
        else if (nt <= 30) { wbase = wq; wstride = DM * 2; }
        else { wbase = wn ? wv : wk; wstride = 36; }
        wbase += 16 * wm * wstride;
        unsigned voffW[5];
#pragma unroll
        for (int p = 0; p < 5; ++p) {
            const int idx = 64 * p + lane, row = idx / 20, c = idx - row * 20;
            int cb;
            if (nt <= 28) cb = (nt < 28 || c < 16) ? c * 16 : 0;
            else if (nt <= 30) {
                const int s10 = c >= 10, cc = c - 10 * s10, hb = 4 * ((nt - 29) * 4 + wn * 2 + s10), hd = hb + (cc >> 1);
                cb = (cc < 8) ? 36 * hd + 4 * (hd & 1) + 16 * (cc & 1) : 36 * (hb + 2 * (cc - 8)) + 32;
            } else cb = (c < 4) ? 16 * (c & 1) + 4 * (c >> 1) : 0;
            voffW[p] = row * wstride + cb;
        }
        const unsigned voffX = (lane >> 3) * (DM * 2) + (((lane & 7) ^ ((4 * (w & 1) + (lane >> 4)) & 7)) << 4);
        const char* xbase = x + (size_t)(mt * 256 + 8 * w) * (DM * 2);
        const unsigned dstX = lds0 + w * 1024, dstW = lds0 + P1_XB + wn * P1_WP + wm * 5120;
        auto dma = [&](int on, int T, int piece) {
            const unsigned buf = (T & 1) ? P1_STAGE : 0;
            if (piece < 4) glds16_if(on, voffX, xbase + piece * (64 * DM * 2) + T * 128, dstX + buf + piece * 8192);
            else glds16_if(on, voffW[piece - 4], wbase + (size_t)T * 64 * wstride, dstW + buf + (piece - 4) * 1024);
        };
#pragma unroll
        for (int p = 0; p < 9; ++p) dma(1, 0, p);
#pragma unroll
        for (int p = 0; p < 9; ++p) dma(1, 1, p);
        if (threadIdx.x < 16) *(LDS_AS unsigned*)(size_t)(lds0 + L_CNT + 4 * threadIdx.x) = 0u;

        unsigned xb0 = lds0 + (64 * wm + 32 * wn + l31) * 128 + (((2 * wm + hi) ^ ((l31 >> 1) & 7)) << 4);
        unsigned xb1 = xb0 + (wn ? -4096 : 4096);
        unsigned wb = lds0 + P1_XB + ((lane >> 4) & 1) * P1_WP + (16 * wm + 8 * hi + ((lane >> 2) & 3)) * 320 + 160 * wn + (lane & 3) * 8;
        const int dW1 = ((1 ^ wm) - wm) * 5120, dW2 = ((2 ^ wm) - (1 ^ wm)) * 5120, dW3 = ((3 ^ wm) - (2 ^ wm)) * 5120, dW0 = (wm - (3 ^ wm)) * 5120;

        f32x16 acc[5][2];
#pragma unroll
        for (int i = 0; i < 5; ++i) { acc[i][0] = f32x16{}; acc[i][1] = f32x16{}; PIN(acc[i][0]); PIN(acc[i][1]); }
        float cs[5] = {0.f, 0.f, 0.f, 0.f, 0.f}, xsum = 0.f, xq0 = 0.f, xq1 = 0.f;
        bf16x8 wf[5], xs[2][2];

        asm volatile("s_waitcnt vmcnt(9) lgkmcnt(0)\n\ts_barrier" ::: "memory");
        xs[0][0] = lds_b128(xb0); xs[0][1] = lds_b128(xb1);
#pragma unroll
        for (int u = 0; u < 4; ++u) wf[u] = lds_tr2(wb + 32 * u, wb + 32 * u + 1280);
        SBAR();

        for (int T = 0; T < 9; ++T) {
            const int dStage = (T & 1) ? -P1_STAGE : P1_STAGE;
            const int more = 7 - T;
#pragma unroll
            for (int U = 0; U < 20; ++U) {
                const int i = U / 5, u = U % 5;
                if (U == 1) wb += dW1;
                if (U == 6) wb += dW2;
                if (U == 11) wb += dW3;
                if (U == 16) {
                    asm volatile("s_waitcnt vmcnt(0) lgkmcnt(0)\n\ts_barrier" ::: "memory");
                    wb += dW0 + dStage; xb0 = (xb0 ^ 96) + dStage; xb1 = (xb1 ^ 96) + dStage;
                    xs[0][0] = lds_b128(xb0); xs[0][1] = lds_b128(xb1);
                }
                wf[(U + 4) % 5] = lds_tr2(wb + 32 * ((U + 4) % 5), wb + 32 * ((U + 4) % 5) + 1280);
                if (u == 1 && i < 3) { xb0 ^= (i ^ (i + 1)) << 5; xs[(i + 1) & 1][0] = lds_b128(xb0); }
                if (u == 2 && i < 3) { xb1 ^= (i ^ (i + 1)) << 5; xs[(i + 1) & 1][1] = lds_b128(xb1); }
                if (U >= 16) { dma(more, T + 2, 2 * (U - 16)); dma(more, T + 2, 2 * (U - 16) + 1); if (U == 19) dma(more, T + 2, 8); }
                const bool docs = (U == 0 || U == 4 || U == 6 || U == 12 || U == 18);
                const int rd = docs ? -1 : (U < 4) ? U - 1 : (U < 10) ? (U == 5 ? 0 : U - 6) : (U < 15) ? (U < 12 ? U - 10 : U - 11) : (U < 18 ? U - 15 : 3);
                const u32x4 wu = __builtin_bit_cast(u32x4, wf[U % 5]), xu = __builtin_bit_cast(u32x4, xs[i & 1][0]);
                acc[u][0] = MFMA32(wf[U % 5], xs[i & 1][0], acc[u][0]);
                SBAR();
                if (docs) col_acc(wu[0], wu[1], cs[u]);
                else row_acc(xu[rd], xsum, xq0, xq1);
                SBAR();
                acc[u][1] = MFMA32(wf[U % 5], xs[i & 1][1], acc[u][1]);
                SBAR();
                if (docs) col_acc(wu[2], wu[3], cs[u]);
                if (U == 3) row_acc(xu[3], xsum, xq0, xq1);
                SBAR();
            }
        }
        PIN(tag);

        const float rsum = sum32(xsum), rsq = sum32(xq0 + xq1);
        if (hi == 0) *(LDS_AS f32x2*)(size_t)(lds0 + L_ST + (w * 32 + l31) * 8) = f32x2{rsum, rsq};
#pragma unroll
        for (int u = 0; u < 5; ++u) {
            const float h = bf16r(cs[u]);
            *(LDS_AS unsigned*)(size_t)(lds0 + L_CS + ((wn * 5 + u) * 64 + lane) * 16 + wm * 4) = pack2(h, cs[u] - h);
        }
        asm volatile("s_waitcnt lgkmcnt(0)\n\ts_barrier" ::: "memory");
        float rstd[2];
        {
            const f32x2 ot = *(const LDS_AS f32x2*)(size_t)(lds0 + L_ST + ((w ^ 4) * 32 + l31) * 8);
            bf16x8 nm[2];
#pragma unroll
            for (int m = 0; m < 2; ++m) {
                const float mu = (m ? ot[0] : rsum) * (1.f / DM);
                const float var = fmaxf((m ? ot[1] : rsq) * (1.f / DM) - mu * mu, 0.f);
                rstd[m] = 1.f / bf16r(sqrtf(bf16r(bf16r(var) + 1e-5f)));
                const unsigned d = pack2(-mu, -mu);
                nm[m] = __builtin_bit_cast(bf16x8, u32x4{d, d, d, d});
            }
#pragma unroll
            for (int u = 0; u < 5; ++u) {
                const bf16x8 cf = lds_b128(lds0 + L_CS + ((wn * 5 + u) * 64 + lane) * 16);
                acc[u][0] = MFMA32(cf, nm[0], acc[u][0]);
                acc[u][1] = MFMA32(cf, nm[1], acc[u][1]);
            }
        }
        tag = ((u64)__builtin_amdgcn_readfirstlane((unsigned)(tag >> 32)) << 32 | __builtin_amdgcn_readfirstlane((unsigned)tag)) + 0x9e3779b97f4a7c15ull;
        if (w < 4) {
            const char* w2b = w2 + ((size_t)(sp * 1152 + 32 * w) * DM + nt2 * 144) * 2;
#pragma unroll
            for (int p = 0; p < 9; ++p) {
                const int idx = 64 * p + lane, r = idx / 18;
                const unsigned vo = ((r & 16) + kperm(r & 15)) * (DM * 2) + (idx - r * 18) * 16;
#pragma unroll
                for (int T = 0; T < 3; ++T) glds16(vo, w2b + (size_t)T * 128 * DM * 2, lds0 + T * G_STAGE + w * 9216 + p * 1024);
            }
        }
        const int tb0 = mt * 8 + 2 * wm;
        if (nt <= 28) {
#pragma unroll
            for (int m = 0; m < 2; ++m) {
                const float er = -1.4426950408889634f * rstd[m], ir2 = 1.f / (rstd[m] * rstd[m]);
                const unsigned so = WS_H + ((tb0 + (m ^ wn)) * 288 + nt * 10 + 5 * wn) * 1024;
#pragma unroll
                for (int u = 0; u < 5; ++u) {
                    u32x4 pk;
#pragma unroll
                    for (int j = 0; j < 4; ++j) {
                        float hv[2];
#pragma unroll
                        for (int e = 0; e < 2; ++e) {
                            const float g = acc[u][m][2 * j + e], up = acc[u][m][8 + 2 * j + e];
                            const float ex = __builtin_amdgcn_exp2f(g * er);
                            hv[e] = (g * up) * __builtin_amdgcn_rcpf(fmaf(ex, ir2, ir2));
                        }
                        pk[j] = pack2(hv[0], hv[1]);
                    }
                    if (u < 3 || nt < 28 || wn == 0) st16(pk, rs, lane * 16, so + u * 1024);
                }
            }
        } else if (nt <= 30) {
#pragma unroll
            for (int m = 0; m < 2; ++m) {
                const float s = rstd[m] * (0.23570226039551584f * 1.4426950408889634f);
                const int tb = tb0 + (m ^ wn);
#pragma unroll
                for (int p = 0; p < 2; ++p) {
                    const int slice = (nt - 29) * 4 + p * 2 + wn;
#pragma unroll
                    for (int u = 0; u < 5; ++u) {
                        u32x4 pk;
#pragma unroll
                        for (int j = 0; j < 4; ++j) pk[j] = pack2(acc[u][m][8 * p + 2 * j] * s, acc[u][m][8 * p + 2 * j + 1] * s);
                        if (u < 4) st16(pk, rs, lane * 16, WS_Q1 + (tb * 32 + slice * 4 + u) * 1024);
                        else if (hi == 0) st16(pk, rs, lane * 16, WS_Q2 + (tb * 8 + slice) * 512);
                    }
                }
            }
        } else if (wn == 0) {
            float kmax = 0.f;
            const unsigned vt = lds0 + L_VT + wm * 2048;
#pragma unroll
            for (int m = 0; m < 2; ++m) {
                const int tb = tb0 + m;
                u32x4 ke, ko, vv;
#pragma unroll
                for (int j = 0; j < 4; ++j) {
                    ke[j] = pack2(acc[0][m][2 * j] * rstd[m], acc[0][m][2 * j + 1] * rstd[m]);
                    ko[j] = pack2(acc[1][m][2 * j] * rstd[m], acc[1][m][2 * j + 1] * rstd[m]);
                    vv[j] = pack2(acc[0][m][8 + 2 * j] * rstd[m], acc[0][m][9 + 2 * j] * rstd[m]);
                }
                float n2 = hi ? blo(ko[3]) * blo(ko[3]) + bhi(ko[3]) * bhi(ko[3]) : 0.f;
#pragma unroll
                for (int j = 0; j < 4; ++j) n2 += blo(ke[j]) * blo(ke[j]) + bhi(ke[j]) * bhi(ke[j]);
                kmax = fmaxf(kmax, sum32(n2));
                st16(ke, rs, lane * 16, WS_K1 + tb * 1024);
                st16(ko, rs, lane * 16, WS_K1 + (64 + tb) * 1024);
                __builtin_amdgcn_raw_buffer_store_b64(u32x2{hi ? ko[3] : ke[0], 0x3f80u}, rs, (hi ? 0 : 64 * 256) + l31 * 8, WS_K2 + tb * 256, SC1);
                *(LDS_AS u32x2*)(size_t)(vt + l31 * 64 + hi * 8) = u32x2{vv[0], vv[1]};
                *(LDS_AS u32x2*)(size_t)(vt + l31 * 64 + 16 + hi * 8) = u32x2{vv[2], vv[3]};
                const unsigned vl = pack2(acc[1][m][14] * rstd[m], acc[1][m][15] * rstd[m]);
                *(LDS_AS u32x4*)(size_t)(vt + l31 * 64 + 48 - hi * 16) = u32x4{hi ? vl : 0u, hi ? 0x3f80u : 0u, 0u, 0u};
                const unsigned va = vt + (4 * hi + ((lane >> 2) & 3)) * 64 + ((lane >> 4) & 1) * 32 + (lane & 3) * 8;
#pragma unroll
                for (int f = 0; f < 2; ++f)
                    st16(__builtin_bit_cast(u32x4, lds_tr2(va + f * 1024, va + f * 1024 + 512)), rs, lane * 16,
                                                           WS_V + (tb * 2 + f) * 1024);
            }
#pragma unroll
            for (int o = 1; o < 32; o <<= 1) kmax = fmaxf(kmax, __shfl_xor(kmax, o));
            if (lane == 0) __builtin_amdgcn_raw_buffer_store_b32(__float_as_uint(kmax), rs, 0, WS_KMAX + (mt * 4 + wm) * 4, SC1);
        }
        asm volatile("s_waitcnt vmcnt(0)" ::: "memory");
        if (cnt_add(lds0 + L_CNT) == 7) flag_set(ws, WS_F1 + (mt * 32 + nt) * 8, tag, lane);
    }
    if (w < 4) {
        const unsigned cG = lds0 + L_CNT + 4, cP = lds0 + L_CNT + 8;
        unsigned voffW[9];
#pragma unroll
        for (int p = 0; p < 9; ++p) { const int idx = 64 * p + lane, r = idx / 18; voffW[p] = ((r & 16) + kperm(r & 15)) * (DM * 2) + (idx - r * 18) * 16; PIN(voffW[p]); }
        const char* w2b = w2 + ((size_t)(sp * 1152 + 32 * w) * DM + nt2 * 144) * 2;
        const char* hb = ws + WS_H + (size_t)((4 * mt2 + w) * 288 + 72 * sp) * 1024;
        const unsigned voffH = lane * 16;
        cnt_post(cG);
        flag_wait(ws, WS_F1 + ((mt2 >> 1) * 32 + 7 * sp + (lane & 7)) * 8, tag);
        bf16x8 hr[24], wf[2][5];
        static_for<0, 24>([&](auto J) { ld16<(J.v & 3) * 1024>(hr[J.v], voffH, hb + (J.v >> 2) * 4096); });
        f32x16 acc[5];
#pragma unroll
        for (int n = 0; n < 5; ++n) acc[n] = f32x16{};
        cnt_wait(cG, 4);
        unsigned ra = lds0 + trl;
#pragma unroll
        for (int n = 0; n < 5; ++n) wf[0][n] = lds_tr2(ra + n * 64, ra + n * 64 + 2304);
        static_for<0, 72>([&](auto JJ) {
            constexpr int J = JJ.v, T = J >> 3, j = J & 7;
            constexpr int nx = ((J + 1) & 7) * 4608;
            if constexpr (j == 7 && T < 8) {
                if constexpr (T + 1 >= 3) asm volatile("s_waitcnt vmcnt(%0) lgkmcnt(0)" :: "n"(g2_after_dma(T + 1)) : "memory");
                else asm volatile("s_waitcnt lgkmcnt(0)" ::: "memory");
                cnt_post(cG);
            }
            waitv<g2_after_h(J)>(hr[J % 24]);
            if constexpr (j != 7) {
#pragma unroll
                for (int n = 0; n < 5; ++n) wf[(J + 1) & 1][n] = lds_tr2(ra + nx + n * 64, ra + nx + n * 64 + 2304);
            }
            SBAR();
            acc[0] = MFMA32(wf[J & 1][0], hr[J % 24], acc[0]);
            acc[1] = MFMA32(wf[J & 1][1], hr[J % 24], acc[1]);
            SBAR();
            if constexpr (j == 7 && T < 8) {
                cnt_wait(cG, 4 * (T + 2));
                ra += (T % 3 == 2) ? -2 * G_STAGE : G_STAGE;
                PIN(ra);
#pragma unroll
                for (int n = 0; n < 5; ++n) wf[(J + 1) & 1][n] = lds_tr2(ra + nx + n * 64, ra + nx + n * 64 + 2304);
                SBAR();
            }
            acc[2] = MFMA32(wf[J & 1][2], hr[J % 24], acc[2]);
            acc[3] = MFMA32(wf[J & 1][3], hr[J % 24], acc[3]);
            acc[4] = MFMA32(wf[J & 1][4], hr[J % 24], acc[4]);
            SBAR();
            if constexpr (g2_dma_at(J) > 0) {
                const char* src = w2b + (size_t)(T + 2) * 128 * DM * 2;
                const unsigned dst = lds0 + ((T + 2) % 3) * G_STAGE + w * 9216;
                if constexpr (j == 0) glds16(voffW[8], src, dst + 8192);
                glds16(voffW[j], src, dst + j * 1024);
            }
            if constexpr (J + 24 < 72) ld16<(J & 3) * 1024>(hr[J % 24], voffH, hb + ((J + 24) >> 2) * 4096);
            SBAR();
        });
        cnt_post(cG);
        if (w != sp) {
#pragma unroll
            for (int c = 0; c < 18; ++c) {
                const f32x16& a = acc[c >> 2];
                const f32x4 v = {a[4 * (c & 3)], a[4 * (c & 3) + 1], a[4 * (c & 3) + 2], a[4 * (c & 3) + 3]};
                st16(__builtin_bit_cast(u32x4, v), rs, lane * 16, WS_P + (bid * 4 + w) * 18432 + c * 1024);
            }
        }
        const unsigned priv = lds0 + w * 27648;
        auto wo_dma = [&](int s0, int piece) {
            const int idx = 64 * piece + lane, r = idx / 18, s = min(9 * w + s0 + (r >> 4), 35), a = (r >> 3) & 1, b = r & 1, c = (r >> 1) & 3;
            const int row = (s < 32) ? 18 * s + 8 * a + 4 * b + c : 18 * (8 * (s - 32) + 4 * b + a + (c & 2)) + 16 + (c & 1);
            glds16(row * (DM * 2) + (idx - r * 18) * 16, wo + nt2 * 288, priv + piece * 1024);
        };
        cnt_wait(cG, 40);
#pragma unroll 1
        for (int p = 0; p < 14; ++p) wo_dma(0, p);
        asm volatile("s_waitcnt vmcnt(0)" ::: "memory");
        if (cnt_add(cP) == 3) flag_set(ws, WS_F2 + bid * 8, tag, lane);
        bf16x8 wr[3][5];
        unsigned rp = priv + trl;
        PIN(rp);
#pragma unroll
        for (int s = 0; s < 3; ++s)
#pragma unroll
            for (int n = 0; n < 5; ++n) wr[s][n] = lds_tr2(rp + s * 4608 + n * 64, rp + s * 4608 + n * 64 + 2304);
        asm volatile("s_waitcnt lgkmcnt(0)" ::: "memory");
#pragma unroll
        for (int s = 0; s < 3; ++s)
#pragma unroll
            for (int n = 0; n < 5; ++n) PIN(wr[s][n]);
#pragma unroll 1
        for (int p = 0; p < 27; ++p) wo_dma(3, p);
        const int tbo = 4 * mt2 + sp;
        u32x4 xr[3];
#pragma unroll
        for (int r = 0; r < 3; ++r) {
            const int i = min(w * 64 + lane + 256 * r, 575), tok = i / 18;
            xr[r] = *(const u32x4*)(x + (size_t)(tbo * 32 + tok) * (DM * 2) + nt2 * 288 + (i - tok * 18) * 16);
        }
        if (w != sp) {
            const int pb = (bid & ~3) | w;
            flag_wait(ws, WS_F2 + pb * 8, tag);
#pragma unroll
            for (int c = 0; c < 18; ++c) {
                const f32x4 v = __builtin_bit_cast(f32x4, __builtin_amdgcn_raw_buffer_load_b128(rs, lane * 16, WS_P + (pb * 4 + sp) * 18432 + c * 1024, SC1));
#pragma unroll
                for (int e = 0; e < 4; ++e) acc[c >> 2][4 * (c & 3) + e] = v[e];
            }
        }
        asm volatile("s_waitcnt vmcnt(0)" ::: "memory");
#pragma unroll
        for (int n = 0; n < 5; ++n) PIN(acc[n]);
        flag_wait(ws, WS_F3 + ((tbo >> 1) * 8 + (lane & 7)) * 8, tag);
        bf16x8 of[9];
#pragma unroll
        for (int s = 0; s < 9; ++s)
            of[s] = __builtin_bit_cast(bf16x8, __builtin_amdgcn_raw_buffer_load_b128(rs, lane * 16, WS_O + (tbo * 36 + 9 * w + s) * 1024, SC1));
#pragma unroll
        for (int s = 0; s < 9; ++s)
#pragma unroll
            for (int n = 0; n < 5; ++n) {
                const bf16x8 a = (s < 3) ? wr[s][n] : lds_tr2(rp + (s - 3) * 4608 + n * 64, rp + (s - 3) * 4608 + n * 64 + 2304);
                acc[n] = MFMA32(a, of[s], acc[n]);
            }
#pragma unroll
        for (int c = 0; c < 18; ++c) {
            const f32x16& a = acc[c >> 2];
            *(LDS_AS f32x4*)(size_t)(priv + c * 1040 + lane * 16) = f32x4{a[4 * (c & 3)], a[4 * (c & 3) + 1], a[4 * (c & 3) + 2], a[4 * (c & 3) + 3]};
        }
        asm volatile("s_waitcnt lgkmcnt(0)" ::: "memory");
        cnt_post(cG);
        cnt_wait(cG, 44);
#pragma unroll
        for (int r = 0; r < 3; ++r) {
            const int i = w * 64 + lane + 256 * r;
            if (i < 576) {
                const int tok = i / 18, c = i - tok * 18;
                f32x4 s0 = {blo(xr[r][0]), bhi(xr[r][0]), blo(xr[r][1]), bhi(xr[r][1])}, s1 = {blo(xr[r][2]), bhi(xr[r][2]), blo(xr[r][3]), bhi(xr[r][3])};
#pragma unroll
                for (int v = 0; v < 4; ++v) {
                    s0 += *(const LDS_AS f32x4*)(size_t)(lds0 + v * 27648 + c * 1040 + tok * 16);
                    s1 += *(const LDS_AS f32x4*)(size_t)(lds0 + v * 27648 + c * 1040 + 512 + tok * 16);
                }
                *(u32x4*)(out + (size_t)(tbo * 32 + tok) * (DM * 2) + nt2 * 288 + c * 16) =
                    u32x4{pack2(s0[0], s0[1]), pack2(s0[2], s0[3]), pack2(s1[0], s1[1]), pack2(s1[2], s1[3])};
            }
        }
        if (bid == 0 && w == 0) {
#pragma unroll
            for (int r = 0; r < 4; ++r) flag_wait(ws, WS_F1 + (r * 64 + lane) * 8, tag);
            flag_set(ws, WS_EPOCH, tag - 0x9e3779b97f4a7c15ull + 1, lane);
        }
    } else {
        __builtin_amdgcn_s_setprio(2);
        const int a = w - 4, par = a & 1, kh = a >> 1, slice = bid & 7, tg = bid >> 3;
        flag_wait(ws, WS_F1 + ((lane & 8) ? (tg >> 2) * 32 + 29 + (slice >> 2) : (lane & 7) * 32 + 31) * 8, tag);
        const char* kb = ws + WS_K1 + (par * 64 + kh * 32) * 1024;
        const char* k2b = ws + WS_K2 + (par * 64 + kh * 32) * 256;
        const char* vb = ws + WS_V + kh * 32 * 2048;
        const unsigned vo16 = lane * 16, vo8 = l31 * 8;
        bf16x8 k1[4], v0[4], v1[4];
        s16x4 k2[4];
        ld16<0>(k1[0], vo16, kb); ld8<0>(k2[0], vo8, k2b);
        static_for<0, 3>([&](auto B) {
            ld16<(B.v + 1) * 1024>(k1[B.v + 1], vo16, kb); ld8<(B.v + 1) * 256>(k2[B.v + 1], vo8, k2b);
            ld16<0>(v0[B.v], vo16, vb + B.v * 2048); ld16<1024>(v1[B.v], vo16, vb + B.v * 2048);
        });
        float km = __uint_as_float(__builtin_amdgcn_raw_buffer_load_b32(rs, l31 * 4, WS_KMAX, SC1));
        bf16x8 q1[4];
        s16x4 q2[4];
        unsigned ql[4];
#pragma unroll
        for (int qb = 0; qb < 4; ++qb) {
            const int tb = 2 * tg + (qb & 1), hd = par + 2 * (qb >> 1);
            q1[qb] = __builtin_bit_cast(bf16x8, __builtin_amdgcn_raw_buffer_load_b128(rs, lane * 16, WS_Q1 + (tb * 32 + slice * 4 + hd) * 1024, SC1));
            ql[qb] = __builtin_amdgcn_raw_buffer_load_b32(rs, l31 * 16, WS_Q2 + (tb * 8 + slice) * 512 + hd * 4, SC1);
        }
#pragma unroll
        for (int o = 1; o < 32; o <<= 1) km = fmaxf(km, __shfl_xor(km, o));
        float mrow[4];
        bool safe = true;
#pragma unroll
        for (int qb = 0; qb < 4; ++qb) {
            const u32x4 u = __builtin_bit_cast(u32x4, q1[qb]);
            float n2 = hi ? 0.f : blo(ql[qb]) * blo(ql[qb]) + bhi(ql[qb]) * bhi(ql[qb]);
#pragma unroll
            for (int i = 0; i < 4; ++i) n2 += blo(u[i]) * blo(u[i]) + bhi(u[i]) * bhi(u[i]);
            const float bound = bf16r(sqrtf(sum32(n2) * km) * 1.01f);
            safe = safe && (bound <= SM_SAFE);
            mrow[qb] = bound;
            q2[qb] = __builtin_bit_cast(s16x4, u32x2{hi ? 0u : ql[qb], hi ? 0u : pack2(-bound, 0.f)});
        }
        f32x16 o[4];
#pragma unroll
        for (int qb = 0; qb < 4; ++qb) o[qb] = f32x16{};
        const f32x16 zero = {};
        asm volatile("s_waitcnt vmcnt(0)" : "+v"(k1[0]), "+v"(k1[1]), "+v"(k1[2]), "+v"(k1[3]), "+v"(k2[0]), "+v"(k2[1]), "+v"(k2[2]), "+v"(k2[3]),
                     "+v"(v0[0]), "+v"(v0[1]), "+v"(v0[2]), "+v"(v1[0]), "+v"(v1[1]), "+v"(v1[2]) :: "memory");
        if (__all(safe)) {
            f32x16 s[2];
            unsigned p0[4], p1[4] = {0u, 0u, 0u, 0u};
            v1[3] = v1[0];
            s[0] = MFMA32(k1[0], q1[0], zero); s[0] = MFMA8(k2[0], q2[0], s[0]);
            SBAR();
            for (int t4 = 0; t4 < 32; t4 += 4) {
                static_for<0, 16>([&](auto UU) {
                    constexpr int b = UU.v >> 2, qb = UU.v & 3, bn = (qb == 3) ? (b + 1) & 3 : b, bp = (qb == 0) ? (b + 3) & 3 : b;
                    f32x16& c = s[qb & 1];
                    f32x16& nx = s[(qb + 1) & 1];
                    float e0, e1, e2, e3;
#define EXP2(a, b, i) a = __builtin_amdgcn_exp2f(c[i]); b = __builtin_amdgcn_exp2f(c[i + 1]); PIN(a); PIN(b)
#define CVT2(p, i) p[i] = pack2(e0, e1); p[i + 1] = pack2(e2, e3); PIN(p[i]); PIN(p[i + 1])
                    EXP2(e0, e1, 0);
                    if constexpr (qb == 3) waitv<10>(k1[bn], k2[bn]);
                    SBAR();
                    nx = MFMA32(k1[bn], q1[(qb + 1) & 3], zero);
                    SBAR();
                    EXP2(e2, e3, 2); CVT2(p0, 0); EXP2(e0, e1, 4);
                    SBAR();
                    nx = MFMA8(k2[bn], q2[(qb + 1) & 3], nx);
                    SBAR();
                    if constexpr (qb == 3) {
                        const int tn = min(t4 + b + 4, 31);
                        ld16<0>(k1[b], vo16, kb + tn * 1024); ld8<0>(k2[b], vo8, k2b + tn * 256);
                    }
                    EXP2(e2, e3, 6); CVT2(p0, 2); EXP2(e0, e1, 8);
                    SBAR();
                    o[(qb + 3) & 3] = MFMA32(v1[bp], __builtin_bit_cast(bf16x8, u32x4{p1[0], p1[1], p1[2], p1[3]}), o[(qb + 3) & 3]);
                    SBAR();
                    if constexpr (qb == 1) {
                        const char* p = vb + min(t4 + b + 3, 31) * 2048;
                        ld16<0>(v0[(b + 3) & 3], vo16, p); ld16<1024>(v1[(b + 3) & 3], vo16, p);
                    }
                    EXP2(e2, e3, 10); CVT2(p1, 0); EXP2(e0, e1, 12);
                    if constexpr (qb == 0) waitv<10>(v0[b], v1[b]);
                    SBAR();
                    o[qb] = MFMA32(v0[b], __builtin_bit_cast(bf16x8, u32x4{p0[0], p0[1], p0[2], p0[3]}), o[qb]);
                    SBAR();
                    EXP2(e2, e3, 14); CVT2(p1, 2);
                    SBAR();
                });
            }
            o[3] = MFMA32(v1[3], __builtin_bit_cast(bf16x8, u32x4{p1[0], p1[1], p1[2], p1[3]}), o[3]);
            asm volatile("s_waitcnt vmcnt(0)" : "+v"(k1[0]), "+v"(k1[1]), "+v"(k1[2]), "+v"(k1[3]), "+v"(k2[0]), "+v"(k2[1]), "+v"(k2[2]), "+v"(k2[3]),
                         "+v"(v0[0]), "+v"(v0[1]), "+v"(v0[2]), "+v"(v1[0]), "+v"(v1[1]), "+v"(v1[2]) :: "memory");
        } else {
#pragma unroll
            for (int qb = 0; qb < 4; ++qb) { q2[qb][2] = 0; mrow[qb] = -3.0e38f; }
#pragma unroll 1
            for (int t = 0; t < 32; ++t) {
                ld16<0>(k1[0], vo16, kb + t * 1024); ld8<0>(k2[0], vo8, k2b + t * 256);
                ld16<0>(v0[0], vo16, vb + t * 2048); ld16<1024>(v1[0], vo16, vb + t * 2048);
                waitv<0>(k1[0], k2[0]); waitv<0>(v0[0], v1[0]);
#pragma unroll
                for (int qb = 0; qb < 4; ++qb) {
                    f32x16 c = MFMA32(k1[0], q1[qb], zero);
                    c = MFMA8(k2[0], q2[qb], c);
                    float mx = c[0];
#pragma unroll
                    for (int r = 1; r < 16; ++r) mx = fmaxf(mx, c[r]);
                    const float mn = fmaxf(mrow[qb], max32(mx)), al = __builtin_amdgcn_exp2f(mrow[qb] - mn);
                    mrow[qb] = mn;
                    u32x4 p0, p1;
#pragma unroll
                    for (int r = 0; r < 4; ++r) {
                        p0[r] = pack2(__builtin_amdgcn_exp2f(c[2 * r] - mn), __builtin_amdgcn_exp2f(c[2 * r + 1] - mn));
                        p1[r] = pack2(__builtin_amdgcn_exp2f(c[8 + 2 * r] - mn), __builtin_amdgcn_exp2f(c[9 + 2 * r] - mn));
                    }
#pragma unroll
                    for (int r = 0; r < 16; ++r) o[qb][r] *= al;
                    o[qb] = MFMA32(v0[0], __builtin_bit_cast(bf16x8, p0), o[qb]);
                    o[qb] = MFMA32(v1[0], __builtin_bit_cast(bf16x8, p1), o[qb]);
                }
            }
        }
        const unsigned ex = lds0 + L_EX + lane * 16, cX = lds0 + L_CNT + 16 + 4 * par, cO = lds0 + L_CNT + 12;
#pragma unroll
        for (int j = 0; j < 2; ++j) {
            const f32x16& s = kh ? o[2 * j] : o[2 * j + 1];
            const float mm = kh ? mrow[2 * j] : mrow[2 * j + 1];
            *(LDS_AS f32x4*)(size_t)(ex + a * 6144 + j * 3072) = f32x4{s[0], s[1], s[2], s[3]};
            *(LDS_AS f32x4*)(size_t)(ex + a * 6144 + j * 3072 + 1024) = f32x4{s[4], s[5], s[6], s[7]};
            *(LDS_AS f32x4*)(size_t)(ex + a * 6144 + j * 3072 + 2048) = f32x4{s[8], s[9], s[10], mm};
        }
        asm volatile("s_waitcnt lgkmcnt(0)" ::: "memory");
        cnt_post(cX);
        cnt_wait(cX, 2);
        const int tb = 2 * tg + kh;
        u32x2 lf;
#pragma unroll
        for (int j = 0; j < 2; ++j) {
            const f32x16& s = kh ? o[2 * j + 1] : o[2 * j];
            const float ma = kh ? mrow[2 * j + 1] : mrow[2 * j];
            const unsigned src = ex + (a ^ 2) * 6144 + j * 3072;
            const f32x4 t0 = *(const LDS_AS f32x4*)(size_t)src, t1 = *(const LDS_AS f32x4*)(size_t)(src + 1024), t2 = *(const LDS_AS f32x4*)(size_t)(src + 2048);
            const float mb = t2[3], mm = fmaxf(ma, mb);
            const float fa = __builtin_amdgcn_exp2f(ma - mm), fb = __builtin_amdgcn_exp2f(mb - mm);
            const float inv = 1.f / low32(s[10] * fa + t2[2] * fb), ia = fa * inv, ib = fb * inv;
            const u32x4 pk = {pack2(s[0] * ia + t0[0] * ib, s[1] * ia + t0[1] * ib), pack2(s[2] * ia + t0[2] * ib, s[3] * ia + t0[3] * ib),
                              pack2(s[4] * ia + t1[0] * ib, s[5] * ia + t1[1] * ib), pack2(s[6] * ia + t1[2] * ib, s[7] * ia + t1[3] * ib)};
            st16(pk, rs, lane * 16, WS_O + (tb * 36 + slice * 4 + par + 2 * j) * 1024);
            lf[j] = pack2(s[8] * ia + t2[0] * ib, s[9] * ia + t2[1] * ib);
        }
        if (hi == 0) __builtin_amdgcn_raw_buffer_store_b64(lf, rs, l31 * 16, WS_O + (tb * 36 + 32 + (slice >> 1)) * 1024 + (slice & 1) * 512 + par * 8, SC1);
        asm volatile("s_waitcnt vmcnt(0)" ::: "memory");
        if (cnt_add(cO) == 3) flag_set(ws, WS_F3 + bid * 8, tag, lane);
    }
}

extern "C" void kernel_launch(void* const* d_in, const int* in_sizes, int n_in, void* d_out, int out_size, void* d_ws, size_t ws_size,
                              hipStream_t stream) {
    (void)hipFuncSetAttribute((const void*)k_all, hipFuncAttributeMaxDynamicSharedMemorySize, LDS_BYTES);
    k_all<<<256, 512, LDS_BYTES, stream>>>((const char*)d_in[0], (const char*)d_in[1], (const char*)d_in[2], (const char*)d_in[3],
                                           (const char*)d_in[4], (const char*)d_in[6], (const char*)d_in[7], (const char*)d_in[8],
                                           (char*)d_ws, (char*)d_out);
}
```

```cpp
#include <hip/hip_runtime.h>
#include <stdint.h>

#ifdef __HIP_DEVICE_COMPILE__
#pragma clang attribute push(__attribute__((target("no-packed-fp32-ops"))), apply_to = function)
#endif

typedef __attribute__((ext_vector_type(8))) __bf16 bf16x8;
typedef __attribute__((ext_vector_type(2))) __bf16 bf16x2;
typedef __attribute__((ext_vector_type(4))) short s16x4;
typedef __attribute__((ext_vector_type(8))) short s16x8;
typedef __attribute__((ext_vector_type(4))) float f32x4;
typedef __attribute__((ext_vector_type(2))) float f32x2;
typedef __attribute__((ext_vector_type(16))) float f32x16;
typedef __attribute__((ext_vector_type(4))) unsigned u32x4;
typedef __attribute__((ext_vector_type(2))) unsigned u32x2;
typedef unsigned long long u64;
#define LDS_AS __attribute__((address_space(3)))
#define GLB_AS __attribute__((address_space(1)))

typedef __attribute__((ext_vector_type(3))) unsigned u32x3;

constexpr int DM = 576, FF = 4608;
constexpr unsigned FS = 64;
constexpr unsigned WS_EPOCH = 0, WS_FKV = 4096, WS_FX = 8192, WS_FO = WS_FX + 256 * FS, WS_FH = WS_FO + 1024 * FS, WS_FP = WS_FH + 2048 * FS;
constexpr int FPS = 128;
constexpr unsigned WS_XN = WS_FP + 1024 * 128;
constexpr unsigned WS_H = WS_XN + 64u * 36 * 1024;
constexpr unsigned WS_K1 = WS_H + 64u * 288 * 1024;
constexpr unsigned WS_K2 = WS_K1 + 2u * 64 * 1024;
constexpr unsigned WS_V = WS_K2 + 2u * 64 * 256;
constexpr unsigned WS_O = WS_V + 64u * 2048;
constexpr unsigned WS_P = WS_O + 64u * 36 * 1024;
constexpr unsigned WS_END = WS_P + 1024u * 9216;

constexpr int W_PART = 10368, W_SLOT = 2 * W_PART;
constexpr int L_KV = W_SLOT, L_WQ = L_KV + 2 * 27648;
constexpr int L_XS = L_WQ + 512 * 144;
constexpr int G_X = 64512, G_Y = G_X + 36864, G_Z = 0, G_L = 36864;
constexpr int L_EX = G_Y + 36864;
constexpr int L_CNT = L_EX + 24576;
constexpr int C_L = 0, C_C = 4, C_A = 8, C_S = 12, C_F = 16, C_B = 20, C_Q1 = 28, C_Q2 = 32, C_X = 36, C_P = 44, C_G = 48, C_K = 24, C_TAG = 56;
constexpr int LDS_BYTES = 163840;
static_assert(L_XS + 8 * 1168 <= L_CNT && 3 * W_SLOT <= G_X && L_CNT + 1024 <= LDS_BYTES && G_L + 9216 <= G_X, "lds map");

constexpr float SM_SAFE = 48.f;
constexpr int APRIO = 2;
constexpr unsigned SPIN_MAX = 1u << 21;
constexpr int GPRIO = 0;
constexpr int LOWT = 2;
constexpr int PFN = 16;
constexpr int WQ1 = 2;
constexpr int WQ2 = 4;
constexpr int MPRIO = 3;
constexpr int KVS = 9;
constexpr int NPF = 1;
constexpr u64 TAG_STEP = 0x9e3779b97f4a7c15ull;

#define SBAR() __builtin_amdgcn_sched_barrier(0)
#define MFMA32(a, b, c) __builtin_amdgcn_mfma_f32_32x32x16_bf16(a, b, c, 0, 0, 0)
#define MFMA8(a, b, c) __builtin_amdgcn_mfma_f32_32x32x8bf16_1k(a, b, c, 0, 0, 0)
#define PIN(x) asm volatile("" : "+v"(x))
#define SC1 16

__device__ __forceinline__ float u2f(unsigned u) { return __builtin_bit_cast(float, u); }
__device__ __forceinline__ unsigned f2u(float f) { return __builtin_bit_cast(unsigned, f); }
__device__ __forceinline__ bool all_lanes(bool p) { return __builtin_amdgcn_ballot_w64(!p) == 0; }
template <int N> struct IC { static constexpr int v = N; };
template <int I, int N, class F> __device__ __forceinline__ void static_for(F&& f) {
    if constexpr (I < N) { f(IC<I>{}); static_for<I + 1, N>(f); }
}

__device__ __forceinline__ void set_m0(unsigned lds_base) { asm volatile("s_mov_b32 m0, %0\n\ts_nop 0" :: "s"(lds_base) : "memory"); }
template <int OFF> __device__ __forceinline__ void glds(unsigned voff, const char* sbase) {
    asm volatile("global_load_lds_dwordx4 %0, %1 offset:%2" :: "v"(voff), "s"(sbase), "n"(OFF) : "memory");
}
template <int OFF> __device__ __forceinline__ void ld16(bf16x8& d, unsigned voff, const char* sbase) {
    asm volatile("global_load_dwordx4 %0, %1, %2 offset:%3 sc1" : "=v"(d) : "v"(voff), "s"(sbase), "n"(OFF) : "memory");
}
template <int OFF> __device__ __forceinline__ void ld8(s16x4& d, unsigned voff, const char* sbase) {
    asm volatile("global_load_dwordx2 %0, %1, %2 offset:%3 sc1" : "=v"(d) : "v"(voff), "s"(sbase), "n"(OFF) : "memory");
}
__device__ __forceinline__ void ldv(bf16x8& a, bf16x8& b, unsigned voff, const char* sbase) {
    asm volatile("s_mov_b64 exec, %4\n\tglobal_load_dwordx4 %0, %2, %3 sc1\n\tglobal_load_dwordx4 %1, %2, %3 offset:576 sc1\n\ts_mov_b64 exec, -1"
                 : "+v"(a), "+v"(b) : "v"(voff), "s"(sbase), "s"(0x0003ffff0003ffffull) : "memory");
}
__device__ __forceinline__ void ldflag(u64& d, unsigned voff, const char* sbase) {
    asm volatile("global_load_dwordx2 %0, %1, %2 sc1" : "=v"(d) : "v"(voff), "s"(sbase) : "memory");
}
template <int N, class T> __device__ __forceinline__ void waitv(T& a) { asm volatile("s_waitcnt vmcnt(%1)" : "+v"(a) : "n"(N < 63 ? N : 63) : "memory"); }
template <int N, class T, class U> __device__ __forceinline__ void waitv(T& a, U& b) {
    asm volatile("s_waitcnt vmcnt(%2)" : "+v"(a), "+v"(b) : "n"(N < 63 ? N : 63) : "memory");
}
template <class T> __device__ __forceinline__ void pin(T& a) { asm volatile("" : "+v"(a) :: "memory"); }
template <int N> __device__ __forceinline__ void waitn() { asm volatile("s_waitcnt vmcnt(%0)" :: "n"(N < 63 ? N : 63) : "memory"); }

__device__ __forceinline__ void st16(u32x4 v, __amdgpu_buffer_rsrc_t rs, unsigned voff, unsigned soff) {
    __builtin_amdgcn_raw_buffer_store_b128(v, rs, voff, soff, SC1);
    asm volatile("s_nop 1" :: "v"(v) : "memory");
}
__device__ __forceinline__ bf16x8 lds_tr2(unsigned a0, unsigned a1) {
    s16x4 lo = __builtin_amdgcn_ds_read_tr16_b64_v4i16((LDS_AS s16x4*)(size_t)a0);
    s16x4 hi = __builtin_amdgcn_ds_read_tr16_b64_v4i16((LDS_AS s16x4*)(size_t)a1);
    s16x8 r = __builtin_shufflevector(lo, hi, 0, 1, 2, 3, 4, 5, 6, 7);
    return __builtin_bit_cast(bf16x8, r);
}
__device__ __forceinline__ f32x4 lds_f4(unsigned a) { return *(const LDS_AS f32x4*)(size_t)a; }
__device__ __forceinline__ void lds_st4(unsigned a, float v0, float v1, float v2, float v3) { *(LDS_AS f32x4*)(size_t)a = f32x4{v0, v1, v2, v3}; }
__device__ __forceinline__ unsigned pack2(float lo, float hi) {
    bf16x2 v = {(__bf16)lo, (__bf16)hi};
    return __builtin_bit_cast(unsigned, v);
}
__device__ __forceinline__ float bf16r(float v) { return (float)(__bf16)v; }
__device__ __forceinline__ float blo(unsigned u) { return u2f(u << 16); }
__device__ __forceinline__ float bhi(unsigned u) { return u2f(u & 0xffff0000u); }
__device__ __forceinline__ float sum32(float a) {
    auto r = __builtin_amdgcn_permlane32_swap(f2u(a), f2u(a), false, false);
    return u2f(r[0]) + u2f(r[1]);
}
__device__ __forceinline__ float max32(float a) {
    auto r = __builtin_amdgcn_permlane32_swap(f2u(a), f2u(a), false, false);
    return fmaxf(u2f(r[0]), u2f(r[1]));
}
__device__ __forceinline__ float low32(float a) {
    auto r = __builtin_amdgcn_permlane32_swap(f2u(a), f2u(a), false, false);
    return u2f(r[0]);
}
__device__ __forceinline__ u64 uni64(u64 v) {
    return (u64)(unsigned)__builtin_amdgcn_readfirstlane((unsigned)(v >> 32)) << 32 | (unsigned)__builtin_amdgcn_readfirstlane((unsigned)v);
}
template <int CTRL> __device__ __forceinline__ float dpp(float a) { return u2f(__builtin_amdgcn_mov_dpp(f2u(a), CTRL, 0xf, 0xf, true)); }
__device__ __forceinline__ float half_sum(float a) {
    a += dpp<0xB1>(a); a += dpp<0x4E>(a); a += dpp<0x141>(a); a += dpp<0x140>(a);
    auto r = __builtin_amdgcn_permlane16_swap(f2u(a), f2u(a), false, false);
    return u2f(r[0]) + u2f(r[1]);
}
__device__ __forceinline__ float half_max(float a) {
    a = fmaxf(a, dpp<0xB1>(a)); a = fmaxf(a, dpp<0x4E>(a)); a = fmaxf(a, dpp<0x141>(a)); a = fmaxf(a, dpp<0x140>(a));
    auto r = __builtin_amdgcn_permlane16_swap(f2u(a), f2u(a), false, false);
    return fmaxf(u2f(r[0]), u2f(r[1]));
}
__device__ __forceinline__ unsigned cnt_add(unsigned addr) {
    unsigned old, r;
    asm volatile("s_mov_b64 exec, 1\n\tds_add_rtn_u32 %0, %2, %3\n\ts_waitcnt lgkmcnt(0)\n\tv_readfirstlane_b32 %1, %0\n\ts_mov_b64 exec, -1"
                 : "=&v"(old), "=s"(r) : "v"(addr), "v"(1u) : "memory");
    return r;
}
template <int OFF> __device__ __forceinline__ void cnt_post(unsigned base, unsigned one) {
    asm volatile("s_mov_b64 exec, 1\n\tds_add_u32 %0, %1 offset:%2\n\ts_mov_b64 exec, -1" :: "v"(base), "v"(one), "n"(OFF) : "memory");
}
template <int OFF, int SLEEP = 1> __device__ __forceinline__ void cnt_wait(unsigned base, unsigned target) {
    unsigned v, t, lim = SPIN_MAX * 8;
    asm volatile("1:\n\tds_read_b32 %0, %3 offset:%5\n\ts_waitcnt lgkmcnt(0)\n\tv_readfirstlane_b32 %1, %0\n\ts_cmp_ge_u32 %1, %4\n\ts_cbranch_scc1 2f\n\t"
                 "s_sleep %6\n\ts_sub_u32 %2, %2, 1\n\ts_cbranch_scc0 1b\n2:"
                 : "=&v"(v), "=&s"(t), "+s"(lim) : "v"(base), "s"(target), "n"(OFF), "n"(SLEEP) : "memory", "scc");
}
template <int OFF> __device__ __forceinline__ unsigned cnt_peek(unsigned base) { return *(volatile const LDS_AS unsigned*)(size_t)(base + OFF); }
template <int OFF, int TARGET> __device__ __forceinline__ void cnt_check(unsigned base, unsigned v) {
    unsigned t, lim;
    asm volatile("s_setprio 3\n\tv_readfirstlane_b32 %1, %0\n\ts_setprio %7\n\ts_cmp_ge_u32 %1, %4\n\ts_cbranch_scc1 2f\n\ts_mov_b32 %2, %6\n"
                 "1:\n\tds_read_b32 %0, %3 offset:%5\n\ts_waitcnt lgkmcnt(0)\n\tv_readfirstlane_b32 %1, %0\n\ts_cmp_ge_u32 %1, %4\n\ts_cbranch_scc1 2f\n\t"
                 "s_sub_u32 %2, %2, 1\n\ts_cbranch_scc0 1b\n2:"
                 : "+v"(v), "=&s"(t), "=&s"(lim) : "v"(base), "n"(TARGET), "n"(OFF), "n"(SPIN_MAX * 8), "n"(GPRIO) : "memory", "scc");
}
template <int OFF> __device__ __forceinline__ unsigned add_now(unsigned base) {
    unsigned r;
    asm volatile("s_setprio 3\n\tv_add_u32 %0, %2, %1\n\ts_setprio %3" : "=v"(r) : "v"(base), "n"(OFF), "n"(GPRIO));
    return r;
}
__device__ __forceinline__ void flag_set(char* ws, unsigned off, u64 tag, int lane) {
    if (lane == 0) __hip_atomic_store((GLB_AS u64*)(ws + off), tag, __ATOMIC_RELAXED, __HIP_MEMORY_SCOPE_AGENT);
}
__device__ __forceinline__ void flag_put(const char* ws, unsigned off, u64 tag) {
    unsigned o; u64 t;
    asm volatile("s_mov_b64 exec, 1\n\ts_setprio 3\n\tv_mov_b32 %0, %2\n\tv_mov_b64 %1, %3\n\ts_setprio %5\n\tglobal_store_dwordx2 %0, %1, %4 sc1\n\ts_mov_b64 exec, -1"
                 : "=&v"(o), "=&v"(t) : "s"(off), "s"(tag), "s"(ws), "n"(GPRIO) : "memory");
}
__device__ __forceinline__ void flag_wait(const char* ws, unsigned off, u64 tag) {
    u64 v; unsigned lim = SPIN_MAX;
    asm volatile("1:\n\tglobal_load_dwordx2 %0, %2, %3 sc1\n\ts_waitcnt vmcnt(0)\n\tv_cmp_ne_u64 vcc, %4, %0\n\ts_cmp_eq_u64 vcc, 0\n\ts_cbranch_scc1 2f\n\t"
                 "s_sleep 2\n\ts_sub_u32 %1, %1, 1\n\ts_cbranch_scc0 1b\n2:"
                 : "=&v"(v), "+s"(lim) : "v"(off), "s"(ws), "s"(tag) : "memory", "scc", "vcc");
}
__device__ __forceinline__ void flag_check(const char* ws, unsigned off, u64 tag, u64 v) {
    unsigned lim;
    asm volatile("s_setprio 3\n\tv_cmp_ne_u64 vcc, %4, %0\n\ts_setprio %6\n\ts_cmp_eq_u64 vcc, 0\n\ts_cbranch_scc1 2f\n\ts_mov_b32 %1, %5\n"
                 "1:\n\tglobal_load_dwordx2 %0, %2, %3 sc1\n\ts_waitcnt vmcnt(0)\n\tv_cmp_ne_u64 vcc, %4, %0\n\ts_cmp_eq_u64 vcc, 0\n\ts_cbranch_scc1 2f\n\t"
                 "s_sleep 1\n\ts_sub_u32 %1, %1, 1\n\ts_cbranch_scc0 1b\n2:"
                 : "+v"(v), "=&s"(lim) : "v"(off), "s"(ws), "s"(tag), "n"(SPIN_MAX), "n"(GPRIO) : "memory", "scc", "vcc");
}
__device__ __forceinline__ void swiglu4(unsigned& o0, unsigned& o1, float g0, float g1, float g2, float g3, float u0, float u1, float u2, float u3) {
    float t0, t1, t2, t3, p0, p1, p2, p3;
    asm("v_mul_f32 %2, 0xbfb8aa3b, %10\n\tv_mul_f32 %3, 0xbfb8aa3b, %11\n\tv_mul_f32 %4, 0xbfb8aa3b, %12\n\tv_mul_f32 %5, 0xbfb8aa3b, %13\n\t"
        "v_exp_f32 %2, %2\n\tv_exp_f32 %3, %3\n\tv_exp_f32 %4, %4\n\tv_exp_f32 %5, %5\n\t"
        "v_mul_f32 %6, %10, %14\n\tv_mul_f32 %7, %11, %15\n\tv_mul_f32 %8, %12, %16\n\tv_mul_f32 %9, %13, %17\n\t"
        "v_add_f32 %2, 1.0, %2\n\tv_add_f32 %3, 1.0, %3\n\tv_add_f32 %4, 1.0, %4\n\tv_add_f32 %5, 1.0, %5\n\t"
        "v_rcp_f32 %2, %2\n\tv_rcp_f32 %3, %3\n\tv_rcp_f32 %4, %4\n\tv_rcp_f32 %5, %5\n\t"
        "v_mul_f32 %6, %6, %2\n\tv_mul_f32 %7, %7, %3\n\tv_mul_f32 %8, %8, %4\n\tv_mul_f32 %9, %9, %5\n\t"
        "v_cvt_pk_bf16_f32 %0, %6, %7\n\tv_cvt_pk_bf16_f32 %1, %8, %9"
        : "=v"(o0), "=v"(o1), "=&v"(t0), "=&v"(t1), "=&v"(t2), "=&v"(t3), "=&v"(p0), "=&v"(p1), "=&v"(p2), "=&v"(p3)
        : "v"(g0), "v"(g1), "v"(g2), "v"(g3), "v"(u0), "v"(u1), "v"(u2), "v"(u3));
}

__device__ __forceinline__ int kperm(int r) { return (r & 8) + ((r & 1) << 2) + ((r >> 1) & 3); }

constexpr int S_FA = 42, S_SA = 60, S_CA = 66;
constexpr int g1_nb(int s) { return s < 36 ? 5 : 4; }
constexpr int g1_piece(int s, int u) { const int U = (s & 3) * g1_nb(s) + u; return (U >= 2 && U <= ((s >> 2) <= 15 ? 6 : 10)) ? 1 : 0; }
constexpr int g1_cum(int s, int u) {
    int n = 0;
    for (int t = 0; t <= s; ++t) {
        const int e = t < s ? g1_nb(t) : u;
        if (e > 0 && t == S_FA) ++n;
        for (int i = 0; i < e; ++i) n += g1_piece(t, i);
        if (t < s) n += 2 + (t == S_SA ? 1 : 0) + (t == 35 ? 10 : 0);
    }
    return n;
}
constexpr int g1_upto_loads(int s) { return g1_cum(s + 1, 0) - (s == 35 ? 10 : 0); }
constexpr int g1_upto_unit(int T, int U) { const int nb = g1_nb(4 * T); return g1_cum(4 * T + U / nb, U % nb + 1); }
constexpr int J_FB = 3, J_SB = 18, J_CB = 23, J_SO = 58, J_CO = 63, HR = 16;
constexpr int G2_BASE = g1_cum(72, 0) + 8 + (HR - 12);
constexpr int g2_pieces(int J) { const int j = J & 7; return (J < 64 ? (j >= 1 && j <= 4 ? 2 : j == 5 ? 1 : 0) : 0) + (J == 6 ? 2 : 0); }
constexpr int g2_next(int J) { return J < 72 - HR ? J + HR : (J >= 64 && J <= 72) ? J + 8 : -1; }
constexpr int g2_misc(int J) { return (J == J_FB || J == J_SB || J == J_SO) ? 1 : 0; }
constexpr int g2_cum(int J) { int n = G2_BASE; for (int t = 0; t < J; ++t) n += g2_pieces(t) + g2_misc(t) + (g2_next(t) >= 0 ? 1 : 0); return n; }
constexpr int g2_upto_frag(int J) {
    if (J < 12) return g1_upto_loads(66 + J / 2) - ((J & 1) ? 0 : 1);
    if (J < HR) return G2_BASE - (HR - 1 - J);
    return g2_cum((J < 72 ? J - HR : J - 8) + 1);
}
constexpr int g2_slot(int T) { return T % 3 == 0 ? G_X : T % 3 == 1 ? G_Y : G_Z; }

__global__ __launch_bounds__(512, 2) void k_all(const char* __restrict__ x, const char* __restrict__ wq, const char* __restrict__ wk,
                                                const char* __restrict__ wv, const char* __restrict__ wo, const char* __restrict__ w1,
                                                const char* __restrict__ w3, const char* __restrict__ w2, char* ws, char* __restrict__ out) {
    extern __shared__ __attribute__((aligned(16))) char smem[];
    const unsigned lds0 = (unsigned)(size_t)(LDS_AS char*)smem;
    const int lane = threadIdx.x & 63, w = __builtin_amdgcn_readfirstlane(threadIdx.x >> 6);
    const int bid = blockIdx.x;
    const int l31 = lane & 31, hi = lane >> 5;
    const __amdgpu_buffer_rsrc_t rs = __builtin_amdgcn_make_buffer_rsrc(ws, 0, WS_END, 0x00020000);
    const int th = bid & 1, sp = (bid >> 1) & 3, r_ = bid >> 3;
    const int mt = 4 * th + (r_ >> 3), nt = 8 * sp + (r_ & 7);
    const int mt2 = 8 * th + (r_ >> 2), nt2 = r_ & 3;
    const int tg = 16 * th + (r_ >> 1), slice = 2 * sp + (r_ & 1);
    unsigned cb = lds0 + L_CNT, one = 1u, vo16 = lane * 16;
    PIN(cb); PIN(one); PIN(vo16);
    auto wq_groups = [&](auto G0, auto G1, int v) {
        unsigned vq[3];
#pragma unroll
        for (int i = 0; i < 3; ++i) { const unsigned idx = 64 * (i < 2 ? v + 4 * i : 8) + lane, row = idx / 9; vq[i] = row * (DM * 2) + (idx - row * 9) * 16; }
        const char* qsrc = wq + slice * 144;
#pragma unroll
        for (int g = G0.v; g < G1.v; ++g) {
            set_m0(lds0 + L_WQ + (g * 9 + v) * 1024); glds<0>(vq[0], qsrc + g * 64 * DM * 2);
            set_m0(lds0 + L_WQ + (g * 9 + v + 4) * 1024); glds<0>(vq[1], qsrc + g * 64 * DM * 2);
            if ((g & 3) == 3 || g == 8) {
                const int g2 = g == 8 ? 8 : v + (g & 4);
                set_m0(lds0 + L_WQ + (g2 * 9 + 8) * 1024); glds<0>(vq[2], qsrc + g2 * 64 * DM * 2);
            }
        }
    };
    if (w < 4) {
        __builtin_amdgcn_s_waitcnt(0x0f70);
        if (w == 0 && lane < 14) *(LDS_AS unsigned*)(size_t)(cb + 4 * lane) = 0u;
        asm volatile("s_waitcnt lgkmcnt(0)\n\ts_barrier" ::: "memory");
        const char* wsrc = ((w >> 1) ? w3 : w1) + (size_t)(32 * (w & 1)) * (FF * 2);
        const char* wsrcA = wsrc + 4096 + 160 * nt;
        const char* wsrcB = wsrc + 128 * nt;
        const unsigned dstW = lds0 + (w >> 1) * W_PART + (w & 1) * 5120;
        unsigned voffW[9];
#pragma unroll
        for (int p = 0; p < 5; ++p) { const unsigned idx = 64 * p + lane, row = idx / 10; voffW[p] = row * (FF * 2) + (idx - row * 10) * 16 - (p & 3) * 1024; }
        auto w_piece = [&](auto TT, auto PP) {
            constexpr int T = TT.v, p = PP.v;
            if constexpr ((p & 3) == 0) set_m0(dstW + (T % 3) * W_SLOT + p * 1024);
            glds<(p & 3) * 1024>(voffW[p], T < 9 ? wsrcA + (size_t)T * 64 * FF * 2 : wsrcB + (size_t)(T - 9) * 64 * FF * 2);
        };
        unsigned pf[NPF + 1];
        {
            if (NPF >= 1) {
                const unsigned idx = min(PFN * w + lane, PFN * 4 - 1) % (PFN == 16 ? 54 : 108), row = idx / (PFN == 16 ? 3 : 6);
                const unsigned vo = (18 * r_ + row) * (DM * 2) + ((sp * 288) & ~127) + (idx - row * (PFN == 16 ? 3 : 6)) * (PFN == 16 ? 128 : 64);
                asm volatile("s_mov_b64 exec, %3\n\tglobal_load_dword %0, %1, %2\n\ts_mov_b64 exec, -1" : "=v"(pf[0]) : "v"(vo), "s"(wq), "s"((1ull << PFN) - 1) : "memory");
            }
            if (NPF >= 2) {
                const unsigned idx = min((unsigned)lane, 29u) + 30 * (w & 1), row = idx / 10;
                const unsigned vo = (64 + 6 * r_ + row) * (FF * 2) + 4096 + 1280 * sp + (idx - row * 10) * 128;
                asm volatile("global_load_dword %0, %1, %2" : "=v"(pf[1]) : "v"(vo), "s"((w >> 1) ? w3 : w1) : "memory");
            }
        }
        {
            const char* isrc = ((w >> 1) ? wv : wk) - 2048 + (w & 1) * (256 * 36);
            unsigned vi[3];
#pragma unroll
            for (int p = 0; p < 3; ++p) { const unsigned idx = 64 * p + lane, row = idx / 3, c = idx - row * 3; vi[p] = row * 36 + (c == 2 ? 20 : 16 * c) + 2048 - p * 1024; }
#pragma unroll
            for (int g = 0; g < 5; ++g) {
                set_m0(lds0 + L_KV + (w >> 1) * 27648 + ((w & 1) * 4 + g) * 3072);
                glds<0>(vi[0], isrc + g * 2304); glds<1024>(vi[1], isrc + g * 2304); glds<2048>(vi[2], isrc + g * 2304);
            }
        }
        static_for<0, 5>([&](auto P) { w_piece(IC<0>{}, P); });
        const unsigned wb = lds0 + ((lane >> 4) & 1) * W_PART + (8 * hi + ((lane >> 2) & 3)) * 160 + (lane & 3) * 8;
        f32x16 acc[5][2];
        wq_groups(IC<0>{}, IC<4>{}, w);
        waitn<14>();
        cnt_post<C_K>(cb, one);
        cnt_wait<C_S>(cb, 4);
        const u64 tag = uni64(*(volatile const LDS_AS u64*)(size_t)(cb + C_TAG));
        flag_wait(ws, WS_FX + (mt * 32 + 8 * w + (lane & 7)) * FS, tag);
#pragma unroll
        for (int i = 0; i < NPF; ++i) PIN(pf[i]);
        cnt_post<C_L>(cb, one);
        const int tb0 = mt * 8 + 2 * w;
        const char* xb = ws + WS_XN + (size_t)tb0 * 36864;
        unsigned vo16b = vo16 + 36864;
        PIN(vo16b);
        bf16x8 xr[6][2], wf[6], hr[HR];
        auto x_load = [&](auto SS) {
            constexpr int ks = SS.v % 36;
            ld16<(ks & 3) * 1024>(xr[SS.v % 6][0], vo16, xb + (ks >> 2) * 4096);
            ld16<(ks & 3) * 1024>(xr[SS.v % 6][1], vo16b, xb + (ks >> 2) * 4096);
        };
        static_for<0, 6>([&](auto S) { x_load(S); });
        cnt_wait<C_C>(cb, 4);
        static_for<0, 5>([&](auto P) { w_piece(IC<1>{}, P); });
        cnt_post<C_G>(cb, one);
        const int tbg = 4 * mt2 + w;
        const char* hbA = ws + WS_H + (size_t)(tbg * 288 + 128 + 40 * sp) * 1024;
        const char* hbB = ws + WS_H + (size_t)(tbg * 288 + 32 * sp) * 1024;
        const char* ob = ws + WS_O + (size_t)(tbg * 36) * 1024;
        auto h_load = [&](auto JJ) {
            constexpr int J = JJ.v, r = J < 40 ? J : J < 72 ? J - 40 : J - 72;
            if constexpr (J == 80) ld16<0>(hr[J % HR], vo16, ob + (32 + sp) * 1024);
            else ld16<(r & 3) * 1024>(hr[J % HR], vo16, (J < 40 ? hbA : J < 72 ? hbB : ob + sp * 8192) + (r >> 2) * 4096);
        };
        const char* w2A = w2 + ((size_t)(2048 + 640 * sp + 32 * w) * DM + nt2 * 144) * 2;
        const char* w2B = w2 + ((size_t)(512 * sp + 32 * w) * DM + nt2 * 144) * 2;
        const char* woS = wo + ((size_t)(18 * (8 * sp + 2 * w)) * DM + nt2 * 144) * 2;
        const unsigned dst2 = lds0 + w * 9216;
        auto w2_piece = [&](auto TT, auto PP) {
            constexpr int T = TT.v, p = PP.v, off = (p & 3) * 1024;
            if constexpr (off == 0) set_m0(dst2 + g2_slot(T) + p * 1024);
            if constexpr (T < 9) glds<off>(voffW[p], T < 5 ? w2A + (size_t)T * 128 * DM * 2 : w2B + (size_t)(T - 5) * 128 * DM * 2);
            else if constexpr (p == 4) glds<off>(voffW[p] + hi * (2 * DM * 2), woS);
            else glds<off>(voffW[p], woS + (p > 4 ? 2 * DM * 2 : 0));
        };
        const unsigned fh = WS_FH + ((tbg >> 1) * 32 + 8 * sp + (lane & 7)) * FS;
        u64 fs;
        unsigned pk_c = 0, pk_l = 0;
        cnt_wait<C_L>(cb, 4);
        __builtin_amdgcn_s_setprio(GPRIO);
        const f32x16 zero = {};
        auto epilogue = [&](auto NBB, int ks0) {
            __builtin_amdgcn_s_setprio(3);
#pragma unroll
            for (int m = 0; m < 2; ++m)
#pragma unroll
                for (int u = 0; u < NBB.v; ++u) {
                    const f32x16& c = acc[u][m];
                    unsigned a0, a1, a2, a3;
                    swiglu4(a0, a1, c[0], c[1], c[2], c[3], c[8], c[9], c[10], c[11]);
                    swiglu4(a2, a3, c[4], c[5], c[6], c[7], c[12], c[13], c[14], c[15]);
                    st16(u32x4{a0, a1, a2, a3}, rs, vo16, WS_H + ((tb0 + m) * 288 + ks0 + u) * 1024);
                    SBAR();
                }
            __builtin_amdgcn_s_setprio(GPRIO);
        };
        static_for<0, 72>([&](auto SS) {
            constexpr int s = SS.v, NB = g1_nb(s), NBUF = NB == 5 ? 5 : 6, T = s >> 2, sl = s % 36, UPS = 4 * NB;
            auto w_read = [&](auto GG) {
                constexpr int G = GG.v, st = (s / 36) * 36 + G / NB, off = ((st >> 2) % 3) * W_SLOT + (st & 3) * 2560 + (G % NB) * 32;
                wf[G % NBUF] = lds_tr2(wb + off, wb + off + 640);
            };
            if constexpr (sl == 0) { static_for<0, 4>([&](auto G) { w_read(G); }); SBAR(); }
            static_for<0, NB>([&](auto UU) {
                constexpr int u = UU.v, U = (s & 3) * NB + u, G = sl * NB + u;
                if constexpr (u == 0 && s < 6) waitv<g1_cum(s, 0) + 2 * (5 - s) + 5>(xr[s][0], xr[s][1]);
                if constexpr (u == 0 && s >= 6) {
                    if constexpr (s == S_CA) {
                        asm volatile("s_waitcnt vmcnt(%3)" : "+v"(xr[s % 6][0]), "+v"(xr[s % 6][1]), "+v"(fs) : "n"(g1_cum(s, 0) - g1_upto_loads(s - 6)) : "memory");
                        flag_check(ws, fh, tag, fs);
                    } else waitv<g1_cum(s, 0) - g1_upto_loads(s - 6)>(xr[s % 6][0], xr[s % 6][1]);
                    if constexpr (s == S_FA) flag_put(ws, WS_FH + ((mt * 4 + w) * 32 + nt) * FS, tag);
                }
                if constexpr (T < 17 && U == (NB == 5 ? 12 : 9)) {
                    waitn<g1_cum(s, u) - (T >= 1 ? g1_upto_unit(T - 1, 6) : 0)>();
                    cnt_post<C_L>(cb, one);
                }
                if constexpr (T == 17 && U == 15) { waitn<g1_cum(s, u) - g1_upto_unit(16, 10)>(); cnt_post<C_L>(cb, one); }
                if constexpr (U == 2) cnt_check<C_C, 4 * (T + 1)>(cb, pk_c);
                if constexpr (T == 16 && U == 2) cnt_wait<C_A>(cb, 4);
                if constexpr (T < 17 && T != 8 && U == UPS - 4) cnt_check<C_L, 4 * (T + 2)>(cb, pk_l);
                if constexpr (U == 1) pk_c = cnt_peek<C_C>(cb);
                if constexpr (T < 17 && U == (T == 8 ? UPS - 1 : UPS - 5)) pk_l = cnt_peek<C_L>(cb);
                if constexpr (G + 4 < 36 * NB) w_read(IC<G + 4>{});
                if constexpr (T == 7 && U == 1) {
                    __builtin_amdgcn_s_setprio(3);
                    unsigned ln = lane; PIN(ln);
#pragma unroll
                    for (int p = 0; p < 5; ++p) { const unsigned idx = 64 * p + ln; voffW[p] -= (idx % 10 >= 8) ? 128 : 0; PIN(voffW[p]); }
                    __builtin_amdgcn_s_setprio(GPRIO);
                }
                if constexpr (T == 16 && U == 1) {
                    __builtin_amdgcn_s_setprio(3);
                    unsigned ln = lane; PIN(ln);
#pragma unroll
                    for (int p = 0; p < 9; ++p) {
                        const unsigned idx = 64 * p + ln, r = idx / 18;
                        voffW[p] = ((r & 16) + kperm(r & 15)) * (DM * 2) + (idx - r * 18) * 16 - (p & 3) * 1024;
                        PIN(voffW[p]);
                    }
                    __builtin_amdgcn_s_setprio(GPRIO);
                }
                SBAR();
                acc[u][0] = MFMA32(wf[G % NBUF], xr[s % 6][0], sl == 0 ? zero : acc[u][0]);
                SBAR();
                if constexpr (U == UPS - 1) cnt_post<C_C>(cb, one);
                if constexpr (g1_piece(s, u)) {
                    if constexpr (T <= 15) w_piece(IC<T + 2>{}, IC<U - 2>{});
                    else w2_piece(IC<T - 16>{}, IC<U - 2>{});
                }
                SBAR();
                acc[u][1] = MFMA32(wf[G % NBUF], xr[s % 6][1], sl == 0 ? zero : acc[u][1]);
                SBAR();
            });
            if constexpr (s == S_SA) ldflag(fs, fh, ws);
            if constexpr (s < 66) x_load(IC<s + 6>{});
            else { h_load(IC<2 * (s - 66)>{}); h_load(IC<2 * (s - 66) + 1>{}); }
            SBAR();
            if constexpr (s == 35) {
                epilogue(IC<5>{}, 128 + 5 * nt);
                cnt_check<C_L, 40>(cb, pk_l);
            }
        });
        __builtin_amdgcn_s_setprio(3);
        unsigned trl = lds0 + (2 * ((lane >> 2) & 3) + hi) * 288 + ((lane >> 4) & 1) * 32 + (lane & 3) * 8, ra = trl + G_X;
        unsigned fo = WS_FO + (tbg * 16 + 4 * sp + (lane & 3)) * FS;
        PIN(trl); PIN(ra); PIN(fo);
        cnt_wait<C_L>(cb, 76);
        epilogue(IC<4>{}, 4 * nt);
        static_for<12, HR>([&](auto J) { h_load(J); });
        f32x16 ac[5];
        bf16x8 vf[2][5];
#pragma unroll
        for (int n = 0; n < 5; ++n) vf[0][n] = lds_tr2(ra + n * 64, ra + n * 64 + 2304);
        static_for<0, 81>([&](auto JJ) {
            constexpr int J = JJ.v, T = J >> 3, j = J & 7;
            if constexpr (j == 5 && T < 9) {
                waitn<g2_cum(J) - (T == 0 ? g1_upto_unit(17, 10) : g2_cum(J - 8) + g2_pieces(J - 8))>();
                cnt_post<C_L>(cb, one);
            }
            if constexpr (j == 1 && T < 9) cnt_check<C_C, 4 * (19 + T)>(cb, pk_c);
            if constexpr (j == 7 && T < 9) {
                asm volatile("s_waitcnt lgkmcnt(0)" ::: "memory");
                if constexpr (T < 8) cnt_post<C_C>(cb, one);
                cnt_check<C_L, 4 * (20 + T)>(cb, pk_l);
                ra = add_now<g2_slot(T + 1)>(trl);
            }
            if constexpr (J == 79) ra = add_now<G_L>(trl);
            waitv<g2_cum(J) - g2_upto_frag(J)>(hr[J % HR]);
            if constexpr (j == 0 && T < 9) pk_c = cnt_peek<C_C>(cb);
            if constexpr (j == 6 && T < 9) pk_l = cnt_peek<C_L>(cb);
            SBAR();
            static_for<0, 5>([&](auto NN) {
                constexpr int n = NN.v, nx = ((J + 1) & 7) * 4608 + n * 64;
                if constexpr (J < 80) vf[(J + 1) & 1][n] = lds_tr2(ra + nx, ra + nx + 2304);
                SBAR();
                ac[n] = MFMA32(vf[J & 1][n], hr[J % HR], J == 0 ? zero : ac[n]);
                SBAR();
                if constexpr (J < 64 && j >= 1 && j <= 5 && (n == 1 || (n == 3 && j < 5))) w2_piece(IC<T + 2>{}, IC<2 * (j - 1) + n / 2>{});
                if constexpr (J == 6 && (n == 1 || n == 3)) {
                    __builtin_amdgcn_s_setprio(3);
                    SBAR();
                    unsigned ln = lane; PIN(ln);
                    const unsigned pc = n == 1 ? w : 4, idx = min(64 * pc + ln, 287u), r = idx / 18, a_ = r >> 3, b_ = r & 1, c_ = (r >> 1) & 3;
                    unsigned vo = (18 * (4 * b_ + a_ + (c_ & 2)) + 16 + (c_ & 1)) * (DM * 2) + (idx - r * 18) * 16;
                    PIN(vo);
                    SBAR();
                    __builtin_amdgcn_s_setprio(GPRIO);
                    set_m0(lds0 + G_L + pc * 1024);
                    glds<0>(vo, wo + ((size_t)(144 * sp) * DM + nt2 * 144) * 2);
                }
                SBAR();
            });
            if constexpr (J == J_FB) {
                waitn<g2_cum(J) + g2_pieces(J) - (g1_cum(72, 0) + 8)>();
                flag_put(ws, WS_FH + ((32 + mt * 4 + w) * 32 + nt) * FS, tag);
            }
            if constexpr (J == J_SB) ldflag(fs, fh, ws + 1024 * FS);
            if constexpr (J == J_CB) { waitv<g2_cum(J) + g2_pieces(J) - (g2_cum(J_SB) + g2_pieces(J_SB) + 1)>(fs); flag_check(ws + 1024 * FS, fh, tag, fs); }
            if constexpr (J == J_SO) ldflag(fs, fo, ws);
            if constexpr (J == J_CO) {
                waitv<g2_cum(J) + g2_pieces(J) - (g2_cum(J_SO) + g2_pieces(J_SO) + 1)>(fs);
                flag_check(ws, fo, tag, fs);
            }
            if constexpr (g2_next(J) >= 0) h_load(IC<g2_next(J)>{});
            SBAR();
        });
        __builtin_amdgcn_s_setprio(3);
        if (w != sp) {
            const unsigned dst = ((bid & ~6) | (w << 1)) * 4 + sp;
#pragma unroll
            for (int g = 0; g < 9; ++g) {
                const f32x16& a = ac[g >> 1];
                const int o = 8 * (g & 1);
                auto r0 = __builtin_amdgcn_permlane32_swap(pack2(a[o], a[o + 1]), pack2(a[o + 4], a[o + 5]), false, false);
                auto r1 = __builtin_amdgcn_permlane32_swap(pack2(a[o + 2], a[o + 3]), pack2(a[o + 6], a[o + 7]), false, false);
                st16(u32x4{r0[0], r1[0], r0[1], r1[1]}, rs, vo16, WS_P + dst * 9216 + g * 1024);
            }
            waitn<0>();
            flag_set(ws, WS_FP + dst * FPS, tag, lane);
        } else {
#pragma unroll
            for (int c = 0; c < 18; ++c) {
                const f32x16& a = ac[c >> 2];
                lds_st4(lds0 + G_Y + c * 1024 + l31 * 32 + hi * 16, a[4 * (c & 3)], a[4 * (c & 3) + 1], a[4 * (c & 3) + 2], a[4 * (c & 3) + 3]);
            }
            asm volatile("s_waitcnt lgkmcnt(0)" ::: "memory");
            cnt_post<C_P>(cb, one);
        }
    } else {
        const int a = w - 4, par = a & 1, kh = a >> 1, kh2 = a & 1;
        u64 tag;
        __builtin_amdgcn_s_setprio(3);
        {
            u32x3 r[3];
            const unsigned vx = (2 * a + hi) * (DM * 2) + l31 * 12;
            const char* xs = x + (size_t)bid * 8 * DM * 2;
            asm volatile("global_load_dwordx3 %0, %4, %5\n\tglobal_load_dwordx3 %1, %4, %5 offset:384\n\tglobal_load_dwordx3 %2, %4, %5 offset:768\n\t"
                         "global_load_dwordx2 %3, %6, %7 sc1\n\ts_barrier\n\ts_waitcnt vmcnt(0)"
                         : "=&v"(r[0]), "=&v"(r[1]), "=&v"(r[2]), "=&v"(tag) : "v"(vx), "s"(xs), "v"(WS_EPOCH), "s"(ws) : "memory");
            float v[18], s = 0.f, q = 0.f;
#pragma unroll
            for (int i = 0; i < 9; ++i) { v[2 * i] = blo(r[i / 3][i % 3]); v[2 * i + 1] = bhi(r[i / 3][i % 3]); s += v[2 * i] + v[2 * i + 1]; }
            const float mu = half_sum(s) * (1.f / DM), mub = bf16r(mu);
#pragma unroll
            for (int i = 0; i < 18; ++i) q = fmaf(v[i] - mu, v[i] - mu, q);
            const float rstd = 1.f / bf16r(sqrtf(bf16r(bf16r(half_sum(q) * (1.f / DM)) + 1e-5f)));
            const unsigned dst = lds0 + L_XS + (2 * a + hi) * 1168 + l31 * 12;
#pragma unroll
            for (int i = 0; i < 9; ++i) {
                const unsigned d = pack2(v[2 * i] - mub, v[2 * i + 1] - mub);
                *(LDS_AS unsigned*)(size_t)(dst + (i / 3) * 384 + (i % 3) * 4) = pack2(blo(d) * rstd, bhi(d) * rstd);
            }
            tag = uni64(tag) + TAG_STEP;
            if (a == 0 && lane == 0) *(LDS_AS u64*)(size_t)(cb + C_TAG) = tag;
            asm volatile("s_waitcnt lgkmcnt(0)" ::: "memory");
            cnt_post<C_S>(cb, one);
            cnt_wait<C_S, 0>(cb, 4);
            if (a >= 2) {
#pragma unroll
                for (int i = 0; i < 5; ++i) {
                    const u32x4 d = *(const LDS_AS u32x4*)(size_t)(lds0 + L_XS + (lane & 7) * 1168 + ((4 * kh2 + i) * 8 + (lane >> 3)) * 16);
                    st16(d, rs, (lane >> 3) * 512 + (lane & 7) * 16, WS_XN + (bid >> 2) * 36864 + (bid & 3) * 128 + (4 * kh2 + i) * 4096);
                }
                cnt_post<C_C>(cb, one);
                waitn<0>();
                if (cnt_add(cb + C_B) == 1) flag_set(ws, WS_FX + bid * FS, tag, lane);
            } else {
                const unsigned ba = lds0 + L_KV + a * 27648 + (8 * hi + ((lane >> 2) & 3)) * 48 + ((lane & 16) ? 40 : 8 * (lane & 3));
                const unsigned xa = lds0 + L_XS + (a ? (l31 & 3) | ((l31 >> 1) & 4) : l31 & 7) * 1168 + hi * 16;
                f32x16 c = {};
                bf16x8 fw[5], fx[5];
                auto kv_read = [&](auto I) { fw[I.v % 5] = lds_tr2(ba + I.v * 768, ba + I.v * 768 + 192); fx[I.v % 5] = *(const LDS_AS bf16x8*)(size_t)(xa + I.v * 32); };
                cnt_wait<C_K, 0>(cb, 4);
                static_for<0, 4>(kv_read);
                if (a == 0) {
                    static_for<0, 36>([&](auto I) {
                        if constexpr (I.v + 4 < 36) kv_read(IC<I.v + 4>{});
                        SBAR(); c = MFMA32(fw[I.v % 5], fx[I.v % 5], c); SBAR();
                    });
                    PIN(c);
                    cnt_post<C_C>(cb, one);
                    const unsigned p0 = pack2(c[0], c[1]), p1 = pack2(c[2], c[3]), p2 = pack2(c[4], c[5]), p3 = pack2(c[6], c[7]), p4 = pack2(c[10], c[11]);
                    auto r1 = __builtin_amdgcn_permlane32_swap(p0, p2, false, false);
                    auto r2 = __builtin_amdgcn_permlane32_swap(p2, p4, false, false);
                    const bool odd = l31 & 8;
                    const u32x4 mk = {odd ? p1 : p0, odd ? (hi ? r1[0] : r1[1]) : p1, odd ? p3 : p2, odd ? (hi ? r2[0] : r2[1]) : p3};
                    st16(mk, rs, l31 < 16 ? (odd ? 65536 : 0) + hi * 512 + (l31 & 7) * 16 : 0x80000000u, WS_K1 + (bid >> 2) * 1024 + (bid & 3) * 128);
                    __builtin_amdgcn_raw_buffer_store_b64(u32x2{odd ? p0 : p4, 0x3f80u}, rs, lane < 16 ? (odd ? 16384 : 0) + (l31 & 7) * 8 : 0x80000000u,
                                                          WS_K2 + (bid >> 2) * 256 + (bid & 3) * 64, SC1);
                    float n2 = hi ? 0.f : blo(p4) * blo(p4) + bhi(p4) * bhi(p4);
                    n2 += blo(p0) * blo(p0) + bhi(p0) * bhi(p0) + blo(p1) * blo(p1) + bhi(p1) * bhi(p1);
                    n2 += blo(p2) * blo(p2) + bhi(p2) * bhi(p2) + blo(p3) * blo(p3) + bhi(p3) * bhi(p3);
                    const float kmax = half_max(sum32(n2));
                    waitn<0>();
                    flag_set(ws, WS_FKV + bid * 16, (u64)f2u(kmax) << 32 | (unsigned)tag, lane);
                } else {
                    static_for<0, 36>([&](auto I) {
                        if constexpr (I.v + 4 < 36) kv_read(IC<I.v + 4>{});
                        SBAR(); c = MFMA32(fx[I.v % 5], fw[I.v % 5], c); SBAR();
                    });
                    PIN(c);
                    cnt_post<C_C>(cb, one);
                    const u32x4 mv = {pack2(c[0], c[1]), pack2(c[2], c[3]), pack2(c[4], c[5]), pack2(c[6], c[7])};
                    st16(mv, rs, (lane < 16 || (lane & 62) == 18) ? (lane < 16 ? lane : lane - 2) * 16 : 0x80000000u, WS_V + bid * 288);
                    waitn<0>();
                    flag_set(ws, WS_FKV + bid * 16 + 8, tag, lane);
                }
            }
        }
        const f32x16 zero = {};
        bf16x8 q1[4];
        unsigned ql[4];
        float km;
        const char* kb = ws + WS_K1 + (par * 64 + kh * 32) * 1024;
        const char* k2b = ws + WS_K2 + (par * 64 + kh * 32) * 256;
        const char* vb = ws + WS_V + kh * 32 * 1152;
        unsigned vo8 = ((l31 >> 4) * 2 + ((l31 >> 2) & 1)) * 64 + (((l31 >> 3) & 1) * 4 + (l31 & 3)) * 8, vok = vo8 * 2 + hi * 512;
        unsigned vov = hi * 288 + l31 * 16;
        PIN(vo8); PIN(vok); PIN(vov);
        bf16x8 k1[4], v0[4], v1[4];
        s16x4 k2[4], q2[4];
#pragma unroll
        for (int i = 0; i < 4; ++i) { const unsigned cs = l31 == 18 ? 0x3f803f80u : 0u; v0[i] = v1[i] = __builtin_bit_cast(bf16x8, u32x4{cs, cs, cs, cs}); PIN(v0[i]); PIN(v1[i]); }
        {
            constexpr float QS = 0.23570226039551584f * 1.4426950408889634f;
            const int q4 = lane & 3, g16 = (lane >> 4) & 1, krow = 288 * kh + 8 * hi + ((lane >> 2) & 3);
            bf16x8 bw[9], xf[18];
            flag_wait(ws, WS_FX + (tg * 8 + (lane & 7)) * FS, tag);
            const int tb = 2 * tg + par;
            const char* xb = ws + WS_XN + (size_t)(tb * 36 + 18 * kh) * 1024;
            cnt_wait<C_G>(cb, 4);
            wq_groups(IC<4>{}, IC<9>{}, a);
            static_for<0, 18>([&](auto I) { ld16<(I.v & 3) * 1024>(xf[I.v], vo16, xb + (I.v >> 2) * 4096); });
            const char* fkv = ws + WS_FKV + kh * 2048;
            bf16x8 f0, f1;
            ld16<0>(f0, vo16, fkv); ld16<1024>(f1, vo16, fkv);
            f32x16 c0, c1, c2;
            const unsigned qa = lds0 + L_WQ + krow * 144 + g16 * 72 + q4 * 8, qb2 = lds0 + L_WQ + krow * 144 + 32 + 72 * (q4 & 1);
            auto q_read = [&](auto II) {
                constexpr int o = II.v * 2304, b = (II.v % 3) * 3;
                bw[b] = lds_tr2(qa + o, qa + o + 576); bw[b + 1] = lds_tr2(qa + o + 40, qa + o + 616); bw[b + 2] = lds_tr2(qb2 + o, qb2 + o + 576);
            };
            waitn<20>();
            cnt_post<C_F>(cb, one);
            cnt_wait<C_F, 0>(cb, 4);
            q_read(IC<0>{}); q_read(IC<1>{});
            static_for<0, 18>([&](auto I) {
                constexpr int b = (I.v % 3) * 3;
                if constexpr (I.v + 2 < 18) q_read(IC<I.v + 2>{});
                if constexpr (I.v < KVS) waitv<19 - I.v>(xf[I.v]);
                if constexpr (I.v == KVS) {
                    for (unsigned lim = SPIN_MAX; lim; --lim) {
                        waitv<0>(f0, f1);
                        const u32x4 a0 = __builtin_bit_cast(u32x4, f0), a1 = __builtin_bit_cast(u32x4, f1);
                        const unsigned tl = (unsigned)tag, tu = (unsigned)(tag >> 32);
                        if (all_lanes(a0[0] == tl && a0[2] == tl && a0[3] == tu && a1[0] == tl && a1[2] == tl && a1[3] == tu)) break;
                        ld16<0>(f0, vo16, fkv); ld16<1024>(f1, vo16, fkv);
                    }
                    waitv<0>(f0, f1);
                    km = fmaxf(u2f(__builtin_bit_cast(u32x4, f0)[1]), u2f(__builtin_bit_cast(u32x4, f1)[1]));
                    static_for<KVS, 18>([&](auto J) { pin(xf[J.v]); });
                    ld16<0>(k1[0], vok, kb); ld8<0>(k2[0], vo8, k2b);
                    static_for<0, 3>([&](auto B) {
                        ld16<(B.v + 1) * 1024>(k1[B.v + 1], vok, kb); ld8<(B.v + 1) * 256>(k2[B.v + 1], vo8, k2b);
                        ldv(v0[B.v], v1[B.v], vov, vb + B.v * 1152);
                    });
                }
                SBAR();
                c0 = MFMA32(bw[b], xf[I.v], I.v == 0 ? zero : c0);
                c1 = MFMA32(bw[b + 1], xf[I.v], I.v == 0 ? zero : c1);
                c2 = MFMA32(bw[b + 2], xf[I.v], I.v == 0 ? zero : c2);
                if constexpr (I.v > KVS) pin(c2);
                SBAR();
            });
            cnt_post<C_Q1>(cb, one);
            cnt_wait<C_Q1, 0>(cb, 4);
            const unsigned qe = lds0 + L_WQ + vo16;
#pragma unroll
            for (int i = 0; i < 4; ++i) {
                lds_st4(qe + a * 9216 + i * 1024, c0[4 * i], c0[4 * i + 1], c0[4 * i + 2], c0[4 * i + 3]);
                lds_st4(qe + a * 9216 + 4096 + i * 1024, c1[4 * i], c1[4 * i + 1], c1[4 * i + 2], c1[4 * i + 3]);
            }
            lds_st4(qe + a * 9216 + 8192, c2[0], c2[1], c2[2], c2[3]);
            asm volatile("s_waitcnt lgkmcnt(0)" ::: "memory");
            cnt_post<C_Q2>(cb, one);
            cnt_wait<C_Q2, 0>(cb, 4);
#pragma unroll
            for (int qb = 0; qb < 4; ++qb) {
                const unsigned src = qe + (qb & 1) * 9216 + par * 4096 + (qb >> 1) * 2048;
                const f32x4 t0 = (lds_f4(src) + lds_f4(src + 18432)) * QS, t1 = (lds_f4(src + 1024) + lds_f4(src + 19456)) * QS;
                q1[qb] = __builtin_bit_cast(bf16x8, u32x4{pack2(t0[0], t0[1]), pack2(t0[2], t0[3]), pack2(t1[0], t1[1]), pack2(t1[2], t1[3])});
                const unsigned sl = lds0 + L_WQ + (qb & 1) * 9216 + 8192 + ((qb >> 1) * 32 + l31) * 16 + par * 8;
                const f32x2 t2 = (*(const LDS_AS f32x2*)(size_t)sl + *(const LDS_AS f32x2*)(size_t)(sl + 18432)) * QS;
                ql[qb] = pack2(t2[0], t2[1]);
            }
            asm volatile("s_waitcnt lgkmcnt(0)" ::: "memory");
#pragma unroll
            for (int qb = 0; qb < 4; ++qb) { PIN(q1[qb]); PIN(ql[qb]); }
            cnt_post<C_A>(cb, one);
        }
        __builtin_amdgcn_s_setprio(LOWT ? 0 : APRIO);
        km = max32(half_max(km));
        float mrow[4];
        bool safe = true;
#pragma unroll
        for (int qb = 0; qb < 4; ++qb) {
            const u32x4 u = __builtin_bit_cast(u32x4, q1[qb]);
            float n2 = hi ? 0.f : blo(ql[qb]) * blo(ql[qb]) + bhi(ql[qb]) * bhi(ql[qb]);
#pragma unroll
            for (int i = 0; i < 4; ++i) n2 += blo(u[i]) * blo(u[i]) + bhi(u[i]) * bhi(u[i]);
            const float bound = bf16r(__builtin_amdgcn_sqrtf(sum32(n2) * km) * 1.01f);
            safe = safe && (bound <= SM_SAFE);
            mrow[qb] = bound;
            q2[qb] = __builtin_bit_cast(s16x4, u32x2{hi ? 0u : ql[qb], hi ? 0u : pack2(-bound, 0.f)});
        }
        f32x16 o[4];
#pragma unroll
        for (int qb = 0; qb < 4; ++qb) o[qb] = f32x16{};
        asm volatile("s_waitcnt vmcnt(0)" : "+v"(k1[0]), "+v"(k1[1]), "+v"(k1[2]), "+v"(k1[3]), "+v"(k2[0]), "+v"(k2[1]), "+v"(k2[2]), "+v"(k2[3]),
                     "+v"(v0[0]), "+v"(v0[1]), "+v"(v0[2]), "+v"(v1[0]), "+v"(v1[1]), "+v"(v1[2]) :: "memory");
        if (all_lanes(safe)) {
            f32x16 s[2];
            unsigned p0[4], p1[4] = {0u, 0u, 0u, 0u};
            s[0] = MFMA32(k1[0], q1[0], zero); s[0] = MFMA8(k2[0], q2[0], s[0]);
            SBAR();
            for (int t4 = 0; t4 < 32; t4 += 4) {
                static_for<0, 16>([&](auto UU) {
                    constexpr int b = UU.v >> 2, qb = UU.v & 3, bn = (qb == 3) ? (b + 1) & 3 : b, bp = (qb == 0) ? (b + 3) & 3 : b;
                    f32x16& c = s[qb & 1];
                    f32x16& nx = s[(qb + 1) & 1];
                    float e0, e1, e2, e3;
#define EXP2(a, b, i) a = __builtin_amdgcn_exp2f(c[i]); b = __builtin_amdgcn_exp2f(c[i + 1]); PIN(a); PIN(b)
#define CVT2(p, i) p[i] = pack2(e0, e1); p[i + 1] = pack2(e2, e3); PIN(p[i]); PIN(p[i + 1])
                    if constexpr (LOWT && qb == 0 && b == (LOWT & 3)) { if (t4 == (LOWT & ~3)) __builtin_amdgcn_s_setprio(APRIO); }
                    EXP2(e0, e1, 0);
                    if constexpr (qb == 3) waitv<10>(k1[bn], k2[bn]);
                    SBAR();
                    nx = MFMA32(k1[bn], q1[(qb + 1) & 3], zero);
                    SBAR();
                    EXP2(e2, e3, 2); CVT2(p0, 0); EXP2(e0, e1, 4);
                    SBAR();
                    nx = MFMA8(k2[bn], q2[(qb + 1) & 3], nx);
                    SBAR();
                    if constexpr (qb == 3) {
                        const int tn = min(t4 + b + 4, 31);
                        ld16<0>(k1[b], vok, kb + tn * 1024); ld8<0>(k2[b], vo8, k2b + tn * 256);
                    }
                    EXP2(e2, e3, 6); CVT2(p0, 2); EXP2(e0, e1, 8);
                    SBAR();
                    o[(qb + 3) & 3] = MFMA32(v1[bp], __builtin_bit_cast(bf16x8, u32x4{p1[0], p1[1], p1[2], p1[3]}), o[(qb + 3) & 3]);
                    SBAR();
                    if constexpr (qb == 1) {
                        ldv(v0[(b + 3) & 3], v1[(b + 3) & 3], vov, vb + min(t4 + b + 3, 31) * 1152);
                    }
                    EXP2(e2, e3, 10); CVT2(p1, 0); EXP2(e0, e1, 12);
                    if constexpr (qb == 0) waitv<10>(v0[b], v1[b]);
                    SBAR();
                    o[qb] = MFMA32(v0[b], __builtin_bit_cast(bf16x8, u32x4{p0[0], p0[1], p0[2], p0[3]}), o[qb]);
                    SBAR();
                    EXP2(e2, e3, 14); CVT2(p1, 2);
                    SBAR();
                });
            }
            o[3] = MFMA32(v1[3], __builtin_bit_cast(bf16x8, u32x4{p1[0], p1[1], p1[2], p1[3]}), o[3]);
            asm volatile("s_waitcnt vmcnt(0)" : "+v"(k1[0]), "+v"(k1[1]), "+v"(k1[2]), "+v"(k1[3]), "+v"(k2[0]), "+v"(k2[1]), "+v"(k2[2]), "+v"(k2[3]),
                         "+v"(v0[0]), "+v"(v0[1]), "+v"(v0[2]), "+v"(v1[0]), "+v"(v1[1]), "+v"(v1[2]) :: "memory");
        } else {
#pragma unroll
            for (int qb = 0; qb < 4; ++qb) { q2[qb][2] = 0; mrow[qb] = -3.0e38f; }
#pragma unroll 1
            for (int t = 0; t < 32; ++t) {
                ld16<0>(k1[0], vok, kb + t * 1024); ld8<0>(k2[0], vo8, k2b + t * 256);
                ldv(v0[0], v1[0], vov, vb + t * 1152);
                waitv<0>(k1[0], k2[0]); waitv<0>(v0[0], v1[0]);
#pragma unroll
                for (int qb = 0; qb < 4; ++qb) {
                    f32x16 c = MFMA32(k1[0], q1[qb], zero);
                    c = MFMA8(k2[0], q2[qb], c);
                    float mx = c[0];
#pragma unroll
                    for (int r = 1; r < 16; ++r) mx = fmaxf(mx, c[r]);
                    const float mn = fmaxf(mrow[qb], max32(mx)), al = __builtin_amdgcn_exp2f(mrow[qb] - mn);
                    mrow[qb] = mn;
                    u32x4 p0, p1;
#pragma unroll
                    for (int r = 0; r < 4; ++r) {
                        p0[r] = pack2(__builtin_amdgcn_exp2f(c[2 * r] - mn), __builtin_amdgcn_exp2f(c[2 * r + 1] - mn));
                        p1[r] = pack2(__builtin_amdgcn_exp2f(c[8 + 2 * r] - mn), __builtin_amdgcn_exp2f(c[9 + 2 * r] - mn));
                    }
#pragma unroll
                    for (int r = 0; r < 16; ++r) o[qb][r] *= al;
                    o[qb] = MFMA32(v0[0], __builtin_bit_cast(bf16x8, p0), o[qb]);
                    o[qb] = MFMA32(v1[0], __builtin_bit_cast(bf16x8, p1), o[qb]);
                }
            }
        }
        __builtin_amdgcn_s_setprio(MPRIO);
        const unsigned ex = lds0 + L_EX + vo16;
#pragma unroll
        for (int j = 0; j < 2; ++j) {
            const f32x16& s = kh ? o[2 * j] : o[2 * j + 1];
            const float mm = kh ? mrow[2 * j] : mrow[2 * j + 1];
            lds_st4(ex + a * 6144 + j * 3072, s[0], s[1], s[2], s[3]);
            lds_st4(ex + a * 6144 + j * 3072 + 1024, s[4], s[5], s[6], s[7]);
            lds_st4(ex + a * 6144 + j * 3072 + 2048, s[8], s[9], s[10], mm);
        }
        asm volatile("s_waitcnt lgkmcnt(0)" ::: "memory");
        cnt_post<C_X>(cb + 4 * par, one);
        cnt_wait<C_X, 0>(cb + 4 * par, 2);
        const int tb = 2 * tg + kh;
        u32x2 lf;
#pragma unroll
        for (int j = 0; j < 2; ++j) {
            const f32x16& s = kh ? o[2 * j + 1] : o[2 * j];
            const float ma = kh ? mrow[2 * j + 1] : mrow[2 * j];
            const unsigned src = ex + (a ^ 2) * 6144 + j * 3072;
            const f32x4 t0 = lds_f4(src), t1 = lds_f4(src + 1024), t2 = lds_f4(src + 2048);
            const float mb = t2[3], mm = fmaxf(ma, mb);
            const float fa = __builtin_amdgcn_exp2f(ma - mm), fb = __builtin_amdgcn_exp2f(mb - mm);
            const float inv = __builtin_amdgcn_rcpf(low32(s[10] * fa + t2[2] * fb)), ia = fa * inv, ib = fb * inv;
            const u32x4 pk = {pack2(s[0] * ia + t0[0] * ib, s[1] * ia + t0[1] * ib), pack2(s[2] * ia + t0[2] * ib, s[3] * ia + t0[3] * ib),
                              pack2(s[4] * ia + t1[0] * ib, s[5] * ia + t1[1] * ib), pack2(s[6] * ia + t1[2] * ib, s[7] * ia + t1[3] * ib)};
            st16(pk, rs, vo16, WS_O + (tb * 36 + slice * 4 + par + 2 * j) * 1024);
            lf[j] = pack2(s[8] * ia + t2[0] * ib, s[9] * ia + t2[1] * ib);
        }
        if (hi == 0) __builtin_amdgcn_raw_buffer_store_b64(lf, rs, l31 * 16, WS_O + (tb * 36 + 32 + (slice >> 1)) * 1024 + (slice & 1) * 512 + par * 8, SC1);
        waitn<0>();
        flag_set(ws, WS_FO + (tb * 16 + slice * 2 + par) * FS, tag, lane);
        __builtin_amdgcn_s_setprio(0);
        const size_t orow = (size_t)((4 * mt2 + sp) * 32 + l31) * (DM * 2) + nt2 * 288 + hi * 16;
        const bool own8 = (l31 >> 3) == a;
        u32x4 xr[3];
#pragma unroll
        for (int i = 0; i < 3; ++i) xr[i] = *(const u32x4*)(x + orow + (i < 2 ? a + 4 * i : 8) * 32);
        if (bid == 0 && a == 1) {
#pragma unroll
            for (int r = 0; r < 4; ++r) flag_wait(ws, WS_FX + (r * 64 + lane) * FS, tag);
            flag_set(ws, WS_EPOCH, tag - TAG_STEP + 1, lane);
        }
        cnt_wait<C_P, 4>(cb, 1);
        f32x4 s0[3], s1[3];
#pragma unroll
        for (int i = 0; i < 3; ++i) {
            const unsigned src = lds0 + G_Y + (i < 2 ? a + 4 * i : 8) * 2048 + lane * 32;
            s0[i] = lds_f4(src) + f32x4{blo(xr[i][0]), bhi(xr[i][0]), blo(xr[i][1]), bhi(xr[i][1])};
            s1[i] = lds_f4(src + 16) + f32x4{blo(xr[i][2]), bhi(xr[i][2]), blo(xr[i][3]), bhi(xr[i][3])};
            PIN(s0[i]); PIN(s1[i]);
        }
        flag_wait(ws, WS_FP + (bid * 4 + ((sp + 1 + min(lane, 2)) & 3)) * FPS, tag);
        u32x4 pp[3][3];
#pragma unroll
        for (int r = 0; r < 3; ++r)
#pragma unroll
            for (int i = 0; i < 3; ++i)
                pp[r][i] = __builtin_amdgcn_raw_buffer_load_b128(rs, (i < 2 || own8) ? vo16 : 0x80000000u,
                                                                 WS_P + (bid * 4 + ((sp + 1 + r) & 3)) * 9216 + (i < 2 ? a + 4 * i : 8) * 1024, SC1);
        SBAR();
#pragma unroll
        for (int i = 0; i < 3; ++i) {
#pragma unroll
            for (int r = 0; r < 3; ++r) {
                s0[i] += f32x4{blo(pp[r][i][0]), bhi(pp[r][i][0]), blo(pp[r][i][1]), bhi(pp[r][i][1])};
                s1[i] += f32x4{blo(pp[r][i][2]), bhi(pp[r][i][2]), blo(pp[r][i][3]), bhi(pp[r][i][3])};
            }
            if (i < 2 || own8)
                *(u32x4*)(out + orow + (i < 2 ? a + 4 * i : 8) * 32) = u32x4{pack2(s0[i][0], s0[i][1]), pack2(s0[i][2], s0[i][3]), pack2(s1[i][0], s1[i][1]), pack2(s1[i][2], s1[i][3])};
        }
    }
}

#ifdef __HIP_DEVICE_COMPILE__
#pragma clang attribute pop
#endif

extern "C" void kernel_launch(void* const* d_in, const int* in_sizes, int n_in, void* d_out, int out_size, void* d_ws, size_t ws_size,
                              hipStream_t stream) {
    (void)hipFuncSetAttribute((const void*)k_all, hipFuncAttributeMaxDynamicSharedMemorySize, LDS_BYTES);
    k_all<<<256, 512, LDS_BYTES, stream>>>((const char*)d_in[0], (const char*)d_in[1], (const char*)d_in[2], (const char*)d_in[3],
                                           (const char*)d_in[4], (const char*)d_in[6], (const char*)d_in[7], (const char*)d_in[8],
                                           (char*)d_ws, (char*)d_out);
}
```

```cpp
#include <hip/hip_runtime.h>
#include <stdint.h>

#ifdef __HIP_DEVICE_COMPILE__
#pragma clang attribute push(__attribute__((target("no-packed-fp32-ops"))), apply_to = function)
#endif

typedef __attribute__((ext_vector_type(8))) __bf16 bf16x8;
typedef __attribute__((ext_vector_type(2))) __bf16 bf16x2;
typedef __attribute__((ext_vector_type(4))) short s16x4;
typedef __attribute__((ext_vector_type(8))) short s16x8;
typedef __attribute__((ext_vector_type(4))) float f32x4;
typedef __attribute__((ext_vector_type(2))) float f32x2;
typedef __attribute__((ext_vector_type(16))) float f32x16;
typedef __attribute__((ext_vector_type(4))) unsigned u32x4;
typedef __attribute__((ext_vector_type(2))) unsigned u32x2;
typedef unsigned long long u64;
#define LDS_AS __attribute__((address_space(3)))
#define GLB_AS __attribute__((address_space(1)))

typedef __attribute__((ext_vector_type(3))) unsigned u32x3;

constexpr int DM = 576, FF = 4608;
constexpr unsigned FS = 64;
constexpr unsigned WS_EPOCH = 0, WS_FKV = 4096, WS_FX = 8192, WS_FO = WS_FX + 256 * FS, WS_FH = WS_FO + 1024 * FS, WS_FP = WS_FH + 2048 * FS;
constexpr int FPS = 128;
constexpr unsigned WS_XN = WS_FP + 1024 * 128;
constexpr unsigned WS_H = WS_XN + 64u * 36 * 1024;
constexpr unsigned WS_K1 = WS_H + 64u * 288 * 1024;
constexpr unsigned WS_K2 = WS_K1 + 64u * 1024;
constexpr unsigned WS_V = WS_K2 + 64u * 256;
constexpr unsigned WS_O = WS_V + 64u * 2048;
constexpr unsigned WS_P = WS_O + 64u * 36 * 1024;
constexpr unsigned WS_END = WS_P + 1024u * 9216;

constexpr int W_PART = 10368, W_SLOT = 2 * W_PART;
constexpr int L_KV = W_SLOT, L_WQ = L_KV + 2 * 27648;
constexpr int L_Q = L_WQ + 27648, L_XS = L_Q + 8 * 1280;
constexpr int G_X = 64512, G_Y = G_X + 36864, G_Z = 0, G_L = 36864;
constexpr int L_EX = G_Y + 36864;
constexpr int L_CNT = L_EX + 24576;
constexpr int C_L = 0, C_C = 4, C_A = 8, C_S = 12, C_F = 16, C_B = 20, C_Q = 28, C_X = 36, C_P = 44, C_E = 48, C_K = 24, C_TAG = 56;
constexpr int LDS_BYTES = 163840;
static_assert(L_XS + 8 * 1168 <= L_CNT && 3 * W_SLOT <= G_X && L_CNT + 1024 <= LDS_BYTES && G_L + 9216 <= G_X, "lds map");

constexpr float SM_SAFE = 48.f;
constexpr int APRIO = 2;
constexpr unsigned SPIN_MAX = 1u << 21;
constexpr int GPRIO = 0;
constexpr int LOWT = 0;
constexpr int JSB = 12;
constexpr int KVW = 12;
constexpr int EPA = 2;
constexpr int PDELAY = 8;
constexpr int PSLEEP = 4;
constexpr int JSE = 74;
constexpr int ESLEEP = 10;
constexpr int CPF = 23;
constexpr int MPRIO = 3;
constexpr unsigned EMPTY = 0xffffffffu;
constexpr u64 TAG_STEP = 0x9e3779b97f4a7c15ull;

#define SBAR() __builtin_amdgcn_sched_barrier(0)
#define MFMA32(a, b, c) __builtin_amdgcn_mfma_f32_32x32x16_bf16(a, b, c, 0, 0, 0)
#define MFMA8(a, b, c) __builtin_amdgcn_mfma_f32_32x32x8bf16_1k(a, b, c, 0, 0, 0)
#define PIN(x) asm volatile("" : "+v"(x))
#define SC1 16

__device__ __forceinline__ float u2f(unsigned u) { return __builtin_bit_cast(float, u); }
__device__ __forceinline__ unsigned f2u(float f) { return __builtin_bit_cast(unsigned, f); }
__device__ __forceinline__ bool all_lanes(bool p) { return __builtin_amdgcn_ballot_w64(!p) == 0; }
template <int N> struct IC { static constexpr int v = N; };
template <int I, int N, class F> __device__ __forceinline__ void static_for(F&& f) {
    if constexpr (I < N) { f(IC<I>{}); static_for<I + 1, N>(f); }
}

__device__ __forceinline__ void set_m0(unsigned lds_base) { asm volatile("s_mov_b32 m0, %0\n\ts_nop 0" :: "s"(lds_base) : "memory"); }
template <int OFF> __device__ __forceinline__ void glds(unsigned voff, const char* sbase) {
    asm volatile("global_load_lds_dwordx4 %0, %1 offset:%2" :: "v"(voff), "s"(sbase), "n"(OFF) : "memory");
}
template <int OFF> __device__ __forceinline__ void ld16(bf16x8& d, unsigned voff, const char* sbase) {
    asm volatile("global_load_dwordx4 %0, %1, %2 offset:%3 sc1" : "=v"(d) : "v"(voff), "s"(sbase), "n"(OFF) : "memory");
}
template <int OFF> __device__ __forceinline__ void ld8(s16x4& d, unsigned voff, const char* sbase) {
    asm volatile("global_load_dwordx2 %0, %1, %2 offset:%3 sc1" : "=v"(d) : "v"(voff), "s"(sbase), "n"(OFF) : "memory");
}
__device__ __forceinline__ void ldv(bf16x8& a, bf16x8& b, unsigned voff, const char* sbase) {
    asm volatile("s_mov_b64 exec, %4\n\tglobal_load_dwordx4 %0, %2, %3 sc1\n\tglobal_load_dwordx4 %1, %2, %3 offset:576 sc1\n\ts_mov_b64 exec, -1"
                 : "+v"(a), "+v"(b) : "v"(voff), "s"(sbase), "s"(0x0003ffff0003ffffull) : "memory");
}
__device__ __forceinline__ void ldflag(u64& d, unsigned voff, const char* sbase) {
    asm volatile("global_load_dwordx2 %0, %1, %2 sc1" : "=v"(d) : "v"(voff), "s"(sbase) : "memory");
}
template <int N, class T> __device__ __forceinline__ void waitv(T& a) { asm volatile("s_waitcnt vmcnt(%1)" : "+v"(a) : "n"(N < 63 ? N : 63) : "memory"); }
template <int N, class T, class U> __device__ __forceinline__ void waitv(T& a, U& b) {
    asm volatile("s_waitcnt vmcnt(%2)" : "+v"(a), "+v"(b) : "n"(N < 63 ? N : 63) : "memory");
}
template <class T> __device__ __forceinline__ void pin(T& a) { asm volatile("" : "+v"(a) :: "memory"); }
template <int N> __device__ __forceinline__ void waitn() { asm volatile("s_waitcnt vmcnt(%0)" :: "n"(N < 63 ? N : 63) : "memory"); }

__device__ __forceinline__ void st16(u32x4 v, __amdgpu_buffer_rsrc_t rs, unsigned voff, unsigned soff) {
    __builtin_amdgcn_raw_buffer_store_b128(v, rs, voff, soff, SC1);
    asm volatile("s_nop 1" :: "v"(v) : "memory");
}
__device__ __forceinline__ bf16x8 lds_tr2(unsigned a0, unsigned a1) {
    s16x4 lo = __builtin_amdgcn_ds_read_tr16_b64_v4i16((LDS_AS s16x4*)(size_t)a0);
    s16x4 hi = __builtin_amdgcn_ds_read_tr16_b64_v4i16((LDS_AS s16x4*)(size_t)a1);
    s16x8 r = __builtin_shufflevector(lo, hi, 0, 1, 2, 3, 4, 5, 6, 7);
    return __builtin_bit_cast(bf16x8, r);
}
__device__ __forceinline__ f32x4 lds_f4(unsigned a) { return *(const LDS_AS f32x4*)(size_t)a; }
__device__ __forceinline__ void lds_st4(unsigned a, float v0, float v1, float v2, float v3) { *(LDS_AS f32x4*)(size_t)a = f32x4{v0, v1, v2, v3}; }
__device__ __forceinline__ unsigned pack2(float lo, float hi) {
    bf16x2 v = {(__bf16)lo, (__bf16)hi};
    return __builtin_bit_cast(unsigned, v);
}
__device__ __forceinline__ float bf16r(float v) { return (float)(__bf16)v; }
__device__ __forceinline__ float blo(unsigned u) { return u2f(u << 16); }
__device__ __forceinline__ float bhi(unsigned u) { return u2f(u & 0xffff0000u); }
__device__ __forceinline__ float sum32(float a) {
    auto r = __builtin_amdgcn_permlane32_swap(f2u(a), f2u(a), false, false);
    return u2f(r[0]) + u2f(r[1]);
}
__device__ __forceinline__ float max32(float a) {
    auto r = __builtin_amdgcn_permlane32_swap(f2u(a), f2u(a), false, false);
    return fmaxf(u2f(r[0]), u2f(r[1]));
}
__device__ __forceinline__ float low32(float a) {
    auto r = __builtin_amdgcn_permlane32_swap(f2u(a), f2u(a), false, false);
    return u2f(r[0]);
}
__device__ __forceinline__ u64 uni64(u64 v) {
    return (u64)(unsigned)__builtin_amdgcn_readfirstlane((unsigned)(v >> 32)) << 32 | (unsigned)__builtin_amdgcn_readfirstlane((unsigned)v);
}
template <int CTRL> __device__ __forceinline__ float dpp(float a) { return u2f(__builtin_amdgcn_mov_dpp(f2u(a), CTRL, 0xf, 0xf, true)); }
__device__ __forceinline__ float half_sum(float a) {
    a += dpp<0xB1>(a); a += dpp<0x4E>(a); a += dpp<0x141>(a); a += dpp<0x140>(a);
    auto r = __builtin_amdgcn_permlane16_swap(f2u(a), f2u(a), false, false);
    return u2f(r[0]) + u2f(r[1]);
}
__device__ __forceinline__ float half_max(float a) {
    a = fmaxf(a, dpp<0xB1>(a)); a = fmaxf(a, dpp<0x4E>(a)); a = fmaxf(a, dpp<0x141>(a)); a = fmaxf(a, dpp<0x140>(a));
    auto r = __builtin_amdgcn_permlane16_swap(f2u(a), f2u(a), false, false);
    return fmaxf(u2f(r[0]), u2f(r[1]));
}
__device__ __forceinline__ unsigned cnt_add(unsigned addr) {
    unsigned old, r;
    asm volatile("s_mov_b64 exec, 1\n\tds_add_rtn_u32 %0, %2, %3\n\ts_waitcnt lgkmcnt(0)\n\tv_readfirstlane_b32 %1, %0\n\ts_mov_b64 exec, -1"
                 : "=&v"(old), "=s"(r) : "v"(addr), "v"(1u) : "memory");
    return r;
}
template <int OFF> __device__ __forceinline__ void cnt_post(unsigned base, unsigned one) {
    asm volatile("s_mov_b64 exec, 1\n\tds_add_u32 %0, %1 offset:%2\n\ts_mov_b64 exec, -1" :: "v"(base), "v"(one), "n"(OFF) : "memory");
}
template <int OFF, int SLEEP = 1> __device__ __forceinline__ void cnt_wait(unsigned base, unsigned target) {
    unsigned v, t, lim = SPIN_MAX * 8;
    asm volatile("1:\n\tds_read_b32 %0, %3 offset:%5\n\ts_waitcnt lgkmcnt(0)\n\tv_readfirstlane_b32 %1, %0\n\ts_cmp_ge_u32 %1, %4\n\ts_cbranch_scc1 2f\n\t"
                 "s_sleep %6\n\ts_sub_u32 %2, %2, 1\n\ts_cbranch_scc0 1b\n2:"
                 : "=&v"(v), "=&s"(t), "+s"(lim) : "v"(base), "s"(target), "n"(OFF), "n"(SLEEP) : "memory", "scc");
}
template <int OFF> __device__ __forceinline__ unsigned cnt_peek(unsigned base) { return *(volatile const LDS_AS unsigned*)(size_t)(base + OFF); }
template <int OFF, int TARGET> __device__ __forceinline__ void cnt_check(unsigned base, unsigned v) {
    unsigned t, lim;
    asm volatile("s_setprio 3\n\tv_readfirstlane_b32 %1, %0\n\ts_setprio %7\n\ts_cmp_ge_u32 %1, %4\n\ts_cbranch_scc1 2f\n\ts_mov_b32 %2, %6\n"
                 "1:\n\tds_read_b32 %0, %3 offset:%5\n\ts_waitcnt lgkmcnt(0)\n\tv_readfirstlane_b32 %1, %0\n\ts_cmp_ge_u32 %1, %4\n\ts_cbranch_scc1 2f\n\t"
                 "s_sub_u32 %2, %2, 1\n\ts_cbranch_scc0 1b\n2:"
                 : "+v"(v), "=&s"(t), "=&s"(lim) : "v"(base), "n"(TARGET), "n"(OFF), "n"(SPIN_MAX * 8), "n"(GPRIO) : "memory", "scc");
}
template <int OFF> __device__ __forceinline__ unsigned add_now(unsigned base) {
    unsigned r;
    asm volatile("s_setprio 3\n\tv_add_u32 %0, %2, %1\n\ts_setprio %3" : "=v"(r) : "v"(base), "n"(OFF), "n"(GPRIO));
    return r;
}
__device__ __forceinline__ void flag_set(char* ws, unsigned off, u64 tag, int lane) {
    if (lane == 0) __hip_atomic_store((GLB_AS u64*)(ws + off), tag, __ATOMIC_RELAXED, __HIP_MEMORY_SCOPE_AGENT);
}
__device__ __forceinline__ void flag_put(const char* ws, unsigned off, u64 tag) {
    unsigned o; u64 t;
    asm volatile("s_mov_b64 exec, 1\n\ts_setprio 3\n\tv_mov_b32 %0, %2\n\tv_mov_b64 %1, %3\n\ts_setprio %5\n\tglobal_store_dwordx2 %0, %1, %4 sc1\n\ts_mov_b64 exec, -1"
                 : "=&v"(o), "=&v"(t) : "s"(off), "s"(tag), "s"(ws), "n"(GPRIO) : "memory");
}
__device__ __forceinline__ void flag_wait(const char* ws, unsigned off, u64 tag) {
    u64 v; unsigned lim = SPIN_MAX;
    asm volatile("1:\n\tglobal_load_dwordx2 %0, %2, %3 sc1\n\ts_waitcnt vmcnt(0)\n\tv_cmp_ne_u64 vcc, %4, %0\n\ts_cmp_eq_u64 vcc, 0\n\ts_cbranch_scc1 2f\n\t"
                 "s_sleep 2\n\ts_sub_u32 %1, %1, 1\n\ts_cbranch_scc0 1b\n2:"
                 : "=&v"(v), "+s"(lim) : "v"(off), "s"(ws), "s"(tag) : "memory", "scc", "vcc");
}
__device__ __forceinline__ void flag_check(const char* ws, unsigned off, u64 tag, u64 v) {
    unsigned lim;
    asm volatile("s_setprio 3\n\tv_cmp_ne_u64 vcc, %4, %0\n\ts_setprio %6\n\ts_cmp_eq_u64 vcc, 0\n\ts_cbranch_scc1 2f\n\ts_mov_b32 %1, %5\n"
                 "1:\n\tglobal_load_dwordx2 %0, %2, %3 sc1\n\ts_waitcnt vmcnt(0)\n\tv_cmp_ne_u64 vcc, %4, %0\n\ts_cmp_eq_u64 vcc, 0\n\ts_cbranch_scc1 2f\n\t"
                 "s_sleep 1\n\ts_sub_u32 %1, %1, 1\n\ts_cbranch_scc0 1b\n2:"
                 : "+v"(v), "=&s"(lim) : "v"(off), "s"(ws), "s"(tag), "n"(SPIN_MAX), "n"(GPRIO) : "memory", "scc", "vcc");
}
__device__ __forceinline__ void swiglu4(unsigned& o0, unsigned& o1, float g0, float g1, float g2, float g3, float u0, float u1, float u2, float u3) {
    float t0, t1, t2, t3, p0, p1, p2, p3;
    asm("v_mul_f32 %2, 0xbfb8aa3b, %10\n\tv_mul_f32 %3, 0xbfb8aa3b, %11\n\tv_mul_f32 %4, 0xbfb8aa3b, %12\n\tv_mul_f32 %5, 0xbfb8aa3b, %13\n\t"
        "v_exp_f32 %2, %2\n\tv_exp_f32 %3, %3\n\tv_exp_f32 %4, %4\n\tv_exp_f32 %5, %5\n\t"
        "v_mul_f32 %6, %10, %14\n\tv_mul_f32 %7, %11, %15\n\tv_mul_f32 %8, %12, %16\n\tv_mul_f32 %9, %13, %17\n\t"
        "v_add_f32 %2, 1.0, %2\n\tv_add_f32 %3, 1.0, %3\n\tv_add_f32 %4, 1.0, %4\n\tv_add_f32 %5, 1.0, %5\n\t"
        "v_rcp_f32 %2, %2\n\tv_rcp_f32 %3, %3\n\tv_rcp_f32 %4, %4\n\tv_rcp_f32 %5, %5\n\t"
        "v_mul_f32 %6, %6, %2\n\tv_mul_f32 %7, %7, %3\n\tv_mul_f32 %8, %8, %4\n\tv_mul_f32 %9, %9, %5\n\t"
        "v_cvt_pk_bf16_f32 %0, %6, %7\n\tv_cvt_pk_bf16_f32 %1, %8, %9"
        : "=v"(o0), "=v"(o1), "=&v"(t0), "=&v"(t1), "=&v"(t2), "=&v"(t3), "=&v"(p0), "=&v"(p1), "=&v"(p2), "=&v"(p3)
        : "v"(g0), "v"(g1), "v"(g2), "v"(g3), "v"(u0), "v"(u1), "v"(u2), "v"(u3));
}

__device__ __forceinline__ int kperm(int r) { return (r & 8) + ((r & 1) << 2) + ((r >> 1) & 3); }

constexpr int S_FA = 42, S_SA = 60, S_CA = 66;
constexpr int g1_piece(int s, int u) { const int U = (s & 3) * 5 + u; return (U >= 2 && U <= ((s >> 2) <= 15 ? 6 : 10)) ? 1 : 0; }
constexpr int g1_cum(int s, int u) {
    int n = 0;
    for (int t = 0; t <= s; ++t) {
        const int e = t < s ? 5 : u;
        if (e > 0 && t == S_FA) ++n;
        for (int i = 0; i < e; ++i) n += g1_piece(t, i);
        if (t < s) n += 2 + (t == S_SA ? 1 : 0) + (t == 35 ? 8 : 0);
    }
    return n;
}
constexpr int g1_upto_loads(int s) { return g1_cum(s + 1, 0) - (s == 35 ? 8 : 0); }
constexpr int g1_upto_unit(int T, int U) { return g1_cum(4 * T + U / 5, U % 5 + 1); }
constexpr int J_FB = 3, J_SB = JSB, J_CB = 16, J_SO = 58, J_CO = 63, J_SE = JSE, J_CE = 80, HR = 16;
constexpr int G2_BASE = g1_cum(72, 0) + 10 + (HR - 12);
constexpr int g2_pieces(int J) { const int j = J & 7; return (J < 64 ? (j >= 1 && j <= 4 ? 2 : j == 5 ? 1 : 0) : 0) + (J == 6 ? 2 : 0); }
constexpr int g2_next(int J) { return J < 72 - HR ? J + HR : (J >= 64 && J <= 72) ? J + 8 : -1; }
constexpr int g2_misc(int J) { return (J == J_FB || J == J_SB || J == J_SO || J == J_SE) ? 1 : 0; }
constexpr int g2_cum(int J) { int n = G2_BASE; for (int t = 0; t < J; ++t) n += g2_pieces(t) + g2_misc(t) + (g2_next(t) >= 0 ? 1 : 0); return n; }
constexpr int g2_upto_frag(int J) {
    if (J < 12) return g1_upto_loads(66 + J / 2) - ((J & 1) ? 0 : 1);
    if (J < HR) return G2_BASE - (HR - 1 - J);
    return g2_cum((J < 72 ? J - HR : J - 8) + 1);
}
constexpr int g2_slot(int T) { return T % 3 == 0 ? G_X : T % 3 == 1 ? G_Y : G_Z; }

__global__ __launch_bounds__(512, 2) void k_all(const char* __restrict__ x, const char* __restrict__ wq, const char* __restrict__ wk,
                                                const char* __restrict__ wv, const char* __restrict__ wo, const char* __restrict__ w1,
                                                const char* __restrict__ w3, const char* __restrict__ w2, char* ws, char* __restrict__ out) {
    extern __shared__ __attribute__((aligned(16))) char smem[];
    u64 pc;
    asm volatile("s_getpc_b64 %0" : "=s"(pc));
    const unsigned lds0 = (unsigned)(size_t)(LDS_AS char*)smem;
    const int lane = threadIdx.x & 63, w = __builtin_amdgcn_readfirstlane(threadIdx.x >> 6);
    const int bid = blockIdx.x;
    const int l31 = lane & 31, hi = lane >> 5;
    const __amdgpu_buffer_rsrc_t rs = __builtin_amdgcn_make_buffer_rsrc(ws, 0, WS_END, 0x00020000);
    const int th = bid & 1, sp = (bid >> 1) & 3, r_ = bid >> 3;
    const int mt = 4 * th + (r_ >> 3), nt = 8 * sp + (r_ & 7);
    const int mt2 = 8 * th + (r_ >> 2), nt2 = r_ & 3;
    unsigned pc0;
    if (CPF) {
        const unsigned vo = (r_ * CPF + min(lane, CPF - 1)) * 128;
        asm volatile("s_cmp_lg_u32 %3, 0\n\ts_cbranch_scc1 1f\n\tglobal_load_dword %0, %1, %2\n1:" : "=v"(pc0) : "v"(vo), "s"(pc), "s"(w) : "memory", "scc");
    }
    unsigned cb = lds0 + L_CNT, one = 1u, vo16 = lane * 16;
    PIN(cb); PIN(one); PIN(vo16);
    auto q_groups = [&](int g0, int n) {
        unsigned vi[3];
#pragma unroll
        for (int p = 0; p < 3; ++p) { const unsigned idx = 64 * p + lane, row = idx / 3, c = idx - row * 3; vi[p] = row * (DM * 2) + (c == 2 ? 20 : 16 * c) - p * 1024; }
#pragma unroll
        for (int g = 0; g < n; ++g) {
            const char* src = wq + nt * 36 + (g0 + g) * (64 * DM * 2);
            set_m0(lds0 + L_WQ + (g0 + g) * 3072);
            glds<0>(vi[0], src); glds<1024>(vi[1], src); glds<2048>(vi[2], src);
        }
    };
    if (w < 4) {
        if (w == 0 && lane < 14) *(LDS_AS unsigned*)(size_t)(cb + 4 * lane) = 0u;
        asm volatile("s_waitcnt lgkmcnt(0)\n\ts_barrier" ::: "memory");
        const char* wsrc = ((w >> 1) ? w3 : w1) + (size_t)(32 * (w & 1)) * (FF * 2);
        const char* wsrcA = wsrc + 128 * nt;
        const char* wsrcB = wsrc + 4096 + 160 * nt;
        const unsigned dstW = lds0 + (w >> 1) * W_PART + (w & 1) * 5120;
        unsigned voffW[9];
#pragma unroll
        for (int p = 0; p < 5; ++p) { const unsigned idx = 64 * p + lane, row = idx / 10; voffW[p] = row * (FF * 2) + ((idx - row * 10) & 7) * 16 - (p & 3) * 1024; }
        auto w_piece = [&](auto TT, auto PP) {
            constexpr int T = TT.v, p = PP.v;
            if constexpr ((p & 3) == 0) set_m0(dstW + (T % 3) * W_SLOT + p * 1024);
            glds<(p & 3) * 1024>(voffW[p], T < 9 ? wsrcA + (size_t)T * 64 * FF * 2 : wsrcB + (size_t)(T - 9) * 64 * FF * 2);
        };
        unsigned pf;
        {
            const unsigned idx = min(16 * w + lane, 63) % 54, row = idx / 3;
            const unsigned vo = (18 * r_ + row) * (DM * 2) + ((sp * 288) & ~127) + (idx - row * 3) * 128;
            asm volatile("s_mov_b64 exec, %3\n\tglobal_load_dword %0, %1, %2\n\ts_mov_b64 exec, -1" : "=v"(pf) : "v"(vo), "s"(wq), "s"(0xffffull) : "memory");
        }
        {
            const char* isrc = ((w >> 1) ? wv : wk) - 2048 + (w & 1) * (256 * 36);
            unsigned vi[3];
#pragma unroll
            for (int p = 0; p < 3; ++p) { const unsigned idx = 64 * p + lane, row = idx / 3, c = idx - row * 3; vi[p] = row * 36 + (c == 2 ? 20 : 16 * c) + 2048 - p * 1024; }
#pragma unroll
            for (int g = 0; g < 5; ++g) {
                set_m0(lds0 + L_KV + (w >> 1) * 27648 + ((w & 1) * 4 + g) * 3072);
                glds<0>(vi[0], isrc + g * 2304); glds<1024>(vi[1], isrc + g * 2304); glds<2048>(vi[2], isrc + g * 2304);
            }
        }
        static_for<0, 5>([&](auto P) { w_piece(IC<0>{}, P); });
        const unsigned wb = lds0 + ((lane >> 4) & 1) * W_PART + (8 * hi + ((lane >> 2) & 3)) * 160 + (lane & 3) * 8;
        f32x16 acc[5][2];
        q_groups(w, 1);
        const unsigned wqb = lds0 + L_WQ + (8 * hi + ((lane >> 2) & 3)) * 48 + ((lane & 16) ? 40 : 8 * (lane & 3));
        waitn<8>();
        cnt_post<C_K>(cb, one);
        cnt_wait<C_S>(cb, 4);
        const u64 tag = uni64(*(volatile const LDS_AS u64*)(size_t)(cb + C_TAG));
        flag_wait(ws, WS_FX + (mt * 32 + 8 * w + (lane & 7)) * FS, tag);
        PIN(pf);
        if (CPF) PIN(pc0);
        cnt_post<C_L>(cb, one);
        const int tb0 = mt * 8 + 2 * w;
        const char* xb = ws + WS_XN + (size_t)tb0 * 36864;
        unsigned vo16b = vo16 + 36864;
        PIN(vo16b);
        bf16x8 xr[6][2], wf[5], hr[HR];
        auto x_load = [&](auto SS) {
            constexpr int ks = SS.v % 36;
            ld16<(ks & 3) * 1024>(xr[SS.v % 6][0], vo16, xb + (ks >> 2) * 4096);
            ld16<(ks & 3) * 1024>(xr[SS.v % 6][1], vo16b, xb + (ks >> 2) * 4096);
        };
        static_for<0, 6>([&](auto S) { x_load(S); });
        cnt_wait<C_C>(cb, 4);
        static_for<0, 5>([&](auto P) { w_piece(IC<1>{}, P); });
        const int tbg = 4 * mt2 + w;
        const char* hbA = ws + WS_H + (size_t)(tbg * 288 + 32 * sp) * 1024;
        const char* hbB = ws + WS_H + (size_t)(tbg * 288 + 128 + 40 * sp) * 1024;
        const char* ob = ws + WS_O + (size_t)(tbg * 36) * 1024;
        auto h_load = [&](auto JJ) {
            constexpr int J = JJ.v, r = J < 32 ? J : J < 72 ? J - 32 : J - 72;
            if constexpr (J == 80) ld16<0>(hr[J % HR], vo16, ob + (32 + sp) * 1024);
            else ld16<(r & 3) * 1024>(hr[J % HR], vo16, (J < 32 ? hbA : J < 72 ? hbB : ob + sp * 8192) + (r >> 2) * 4096);
        };
        const char* w2A = w2 + ((size_t)(512 * sp + 32 * w) * DM + nt2 * 144) * 2;
        const char* w2B = w2 + ((size_t)(2048 + 640 * sp + 32 * w) * DM + nt2 * 144) * 2;
        const char* woS = wo + ((size_t)(18 * (8 * sp + 2 * w)) * DM + nt2 * 144) * 2;
        const unsigned dst2 = lds0 + w * 9216;
        auto w2_piece = [&](auto TT, auto PP) {
            constexpr int T = TT.v, p = PP.v, off = (p & 3) * 1024;
            if constexpr (off == 0) set_m0(dst2 + g2_slot(T) + p * 1024);
            if constexpr (T < 9) glds<off>(voffW[p], T < 4 ? w2A + (size_t)T * 128 * DM * 2 : w2B + (size_t)(T - 4) * 128 * DM * 2);
            else if constexpr (p == 4) {
                __builtin_amdgcn_s_setprio(3);
                SBAR();
                unsigned vo = voffW[p] + (vo16 >> 9) * (2 * DM * 2);
                PIN(vo);
                SBAR();
                __builtin_amdgcn_s_setprio(GPRIO);
                glds<off>(vo, woS);
            }
            else glds<off>(voffW[p], woS + (p > 4 ? 2 * DM * 2 : 0));
        };
        const unsigned fh = WS_FH + ((tbg >> 1) * 32 + 8 * sp + (lane & 7)) * FS;
        u64 fs;
        unsigned pk_c = 0, pk_l = 0;
        cnt_wait<C_L>(cb, 4);
        __builtin_amdgcn_s_setprio(GPRIO);
        const f32x16 zero = {};
        auto epilogue = [&](auto NBB, int ks0) {
            __builtin_amdgcn_s_setprio(NBB.v == 4 ? EPA : 3);
#pragma unroll
            for (int m = 0; m < 2; ++m)
#pragma unroll
                for (int u = 0; u < NBB.v; ++u) {
                    const f32x16& c = acc[u + 5 - NBB.v][m];
                    unsigned a0, a1, a2, a3;
                    swiglu4(a0, a1, c[0], c[1], c[2], c[3], c[8], c[9], c[10], c[11]);
                    swiglu4(a2, a3, c[4], c[5], c[6], c[7], c[12], c[13], c[14], c[15]);
                    st16(u32x4{a0, a1, a2, a3}, rs, vo16, WS_H + ((tb0 + m) * 288 + ks0 + u) * 1024);
                    SBAR();
                }
            __builtin_amdgcn_s_setprio(GPRIO);
        };
        static_for<0, 72>([&](auto SS) {
            constexpr int s = SS.v, NB = 5, NBUF = 5, T = s >> 2, sl = s % 36, UPS = 4 * NB;
            auto w_read = [&](auto GG) {
                constexpr int G = GG.v, st = (s / 36) * 36 + G / NB, off = ((st >> 2) % 3) * W_SLOT + (st & 3) * 2560 + (G % NB - (s < 36)) * 32;
                if constexpr (s < 36 && G % NB == 0) wf[G % NBUF] = lds_tr2(wqb + st * 768, wqb + st * 768 + 192);
                else wf[G % NBUF] = lds_tr2(wb + off, wb + off + 640);
            };
            if constexpr (sl == 0) { static_for<0, 4>([&](auto G) { w_read(G); }); SBAR(); }
            static_for<0, NB>([&](auto UU) {
                constexpr int u = UU.v, U = (s & 3) * NB + u, G = sl * NB + u;
                if constexpr (u == 0 && s < 6) waitv<g1_cum(s, 0) + 2 * (5 - s) + 5>(xr[s][0], xr[s][1]);
                if constexpr (u == 0 && s >= 6) {
                    if constexpr (s == S_CA) {
                        asm volatile("s_waitcnt vmcnt(%3)" : "+v"(xr[s % 6][0]), "+v"(xr[s % 6][1]), "+v"(fs) : "n"(g1_cum(s, 0) - g1_upto_loads(s - 6)) : "memory");
                        flag_check(ws, fh, tag, fs);
                    } else waitv<g1_cum(s, 0) - g1_upto_loads(s - 6)>(xr[s % 6][0], xr[s % 6][1]);
                    if constexpr (s == S_FA) flag_put(ws, WS_FH + ((mt * 4 + w) * 32 + nt) * FS, tag);
                }
                if constexpr (T < 17 && U == 12) {
                    waitn<g1_cum(s, u) - (T >= 1 ? g1_upto_unit(T - 1, 6) : 0)>();
                    cnt_post<C_L>(cb, one);
                }
                if constexpr (T == 17 && U == 15) { waitn<g1_cum(s, u) - g1_upto_unit(16, 10)>(); cnt_post<C_L>(cb, one); }
                if constexpr (U == 2) cnt_check<C_C, 4 * (T + 1)>(cb, pk_c);
                if constexpr (T == 3 && U == 3) cnt_wait<C_F>(cb, 2);
                if constexpr (T == 16 && U == 2) cnt_wait<C_A>(cb, 4);
                if constexpr (T < 17 && T != 8 && U == UPS - 4) cnt_check<C_L, 4 * (T + 2)>(cb, pk_l);
                if constexpr (U == 1) pk_c = cnt_peek<C_C>(cb);
                if constexpr (T < 17 && U == (T == 8 ? UPS - 1 : UPS - 5)) pk_l = cnt_peek<C_L>(cb);
                if constexpr (G + 4 < 36 * NB) w_read(IC<G + 4>{});
                if constexpr (T == 7 && U == 1) {
                    __builtin_amdgcn_s_setprio(3);
                    unsigned ln = vo16; PIN(ln); ln >>= 4;
#pragma unroll
                    for (int p = 0; p < 5; ++p) { const unsigned idx = 64 * p + ln; voffW[p] += (idx % 10 >= 8) ? 128 : 0; PIN(voffW[p]); }
                    __builtin_amdgcn_s_setprio(GPRIO);
                }
                if constexpr (T == 16 && U == 1) {
                    __builtin_amdgcn_s_setprio(3);
                    unsigned ln = vo16; PIN(ln); ln >>= 4;
#pragma unroll
                    for (int p = 0; p < 9; ++p) {
                        const unsigned idx = 64 * p + ln, r = idx / 18;
                        voffW[p] = ((r & 16) + kperm(r & 15)) * (DM * 2) + (idx - r * 18) * 16 - (p & 3) * 1024;
                        PIN(voffW[p]);
                    }
                    __builtin_amdgcn_s_setprio(GPRIO);
                }
                SBAR();
                acc[u][0] = MFMA32(wf[G % NBUF], xr[s % 6][0], sl == 0 ? zero : acc[u][0]);
                SBAR();
                if constexpr (U == UPS - 1) cnt_post<C_C>(cb, one);
                if constexpr (g1_piece(s, u)) {
                    if constexpr (T <= 15) w_piece(IC<T + 2>{}, IC<U - 2>{});
                    else w2_piece(IC<T - 16>{}, IC<U - 2>{});
                }
                SBAR();
                acc[u][1] = MFMA32(wf[G % NBUF], xr[s % 6][1], sl == 0 ? zero : acc[u][1]);
                SBAR();
                if constexpr (s == 35 && u == 1) {
                    constexpr float QS = 0.23570226039551584f * 1.4426950408889634f;
                    __builtin_amdgcn_s_setprio(3);
#pragma unroll
                    for (int m = 0; m < 2; ++m) {
                        const f32x16& c = acc[0][m];
                        const unsigned dq = lds0 + L_Q + (2 * w + m) * 1280;
                        *(LDS_AS u32x4*)(size_t)(dq + vo16) = u32x4{pack2(c[0] * QS, c[1] * QS), pack2(c[2] * QS, c[3] * QS), pack2(c[4] * QS, c[5] * QS), pack2(c[6] * QS, c[7] * QS)};
                        *(LDS_AS unsigned*)(size_t)(dq + 1024 + (vo16 >> 2)) = pack2(c[10] * QS, c[11] * QS);
                    }
                    asm volatile("s_waitcnt lgkmcnt(0)" ::: "memory");
                    cnt_post<C_Q>(cb + 4 * (w >> 1), one);
                    __builtin_amdgcn_s_setprio(GPRIO);
                    SBAR();
                }
            });
            if constexpr (s == S_SA) ldflag(fs, fh, ws);
            if constexpr (s < 66) x_load(IC<s + 6>{});
            else { h_load(IC<2 * (s - 66)>{}); h_load(IC<2 * (s - 66) + 1>{}); }
            SBAR();
            if constexpr (s == 35) {
                epilogue(IC<4>{}, 4 * nt);
                cnt_check<C_L, 40>(cb, pk_l);
            }
        });
        __builtin_amdgcn_s_setprio(3);
        unsigned ln = vo16; PIN(ln); ln >>= 4;
        unsigned trl = lds0 + (2 * ((ln >> 2) & 3) + (ln >> 5)) * 288 + ((ln >> 4) & 1) * 32 + (ln & 3) * 8, ra = trl + G_X;
        unsigned fo = WS_FO + (((tbg >> 3) * 32 + 8 * sp + (ln & 7)) * 4 + ((tbg >> 2) & 1) + 2 * (tbg & 1)) * FS;
        const unsigned dst = (bid & ~6) | (w << 1);
        const char* fe = ws + WS_FP + dst * FPS - 1;
        PIN(trl); PIN(ra); PIN(fo);
        cnt_wait<C_L>(cb, 76);
        epilogue(IC<5>{}, 128 + 5 * nt);
        static_for<12, HR>([&](auto J) { h_load(J); });
        f32x16 ac[5];
        bf16x8 vf[2][5];
#pragma unroll
        for (int n = 0; n < 5; ++n) vf[0][n] = lds_tr2(ra + n * 64, ra + n * 64 + 2304);
        static_for<0, 81>([&](auto JJ) {
            constexpr int J = JJ.v, T = J >> 3, j = J & 7;
            if constexpr (j == 5 && T < 9) {
                waitn<g2_cum(J) - (T == 0 ? g1_upto_unit(17, 10) : g2_cum(J - 8) + g2_pieces(J - 8))>();
                cnt_post<C_L>(cb, one);
            }
            if constexpr (j == 1 && T < 9) cnt_check<C_C, 4 * (19 + T)>(cb, pk_c);
            if constexpr (j == 7 && T < 9) {
                asm volatile("s_waitcnt lgkmcnt(0)" ::: "memory");
                if constexpr (T < 8) cnt_post<C_C>(cb, one);
                cnt_check<C_L, 4 * (20 + T)>(cb, pk_l);
                ra = add_now<g2_slot(T + 1)>(trl);
            }
            if constexpr (J == 79) ra = add_now<G_L>(trl);
            waitv<g2_cum(J) - g2_upto_frag(J)>(hr[J % HR]);
            if constexpr (j == 0 && T < 9) pk_c = cnt_peek<C_C>(cb);
            if constexpr (j == 6 && T < 9) pk_l = cnt_peek<C_L>(cb);
            SBAR();
            static_for<0, 5>([&](auto NN) {
                constexpr int n = NN.v, nx = ((J + 1) & 7) * 4608 + n * 64;
                if constexpr (J < 80) vf[(J + 1) & 1][n] = lds_tr2(ra + nx, ra + nx + 2304);
                SBAR();
                ac[n] = MFMA32(vf[J & 1][n], hr[J % HR], J == 0 ? zero : ac[n]);
                SBAR();
                if constexpr (J < 64 && j >= 1 && j <= 5 && (n == 1 || (n == 3 && j < 5))) w2_piece(IC<T + 2>{}, IC<2 * (j - 1) + n / 2>{});
                if constexpr (J == 6 && (n == 1 || n == 3)) {
                    __builtin_amdgcn_s_setprio(3);
                    SBAR();
                    unsigned ln = vo16; PIN(ln); ln >>= 4;
                    const unsigned pc = n == 1 ? w : 4, idx = min(64 * pc + ln, 287u), r = idx / 18, a_ = r >> 3, b_ = r & 1, c_ = (r >> 1) & 3;
                    unsigned vo = (18 * (4 * b_ + a_ + (c_ & 2)) + 16 + (c_ & 1)) * (DM * 2) + (idx - r * 18) * 16;
                    PIN(vo);
                    SBAR();
                    __builtin_amdgcn_s_setprio(GPRIO);
                    set_m0(lds0 + G_L + pc * 1024);
                    glds<0>(vo, wo + ((size_t)(144 * sp) * DM + nt2 * 144) * 2);
                }
                SBAR();
            });
            if constexpr (J == J_FB) {
                waitn<g2_cum(J) + g2_pieces(J) - (g1_cum(72, 0) + 10)>();
                flag_put(ws, WS_FH + ((32 + mt * 4 + w) * 32 + nt) * FS, tag);
            }
            if constexpr (J == J_SB) ldflag(fs, fh, ws + 1024 * FS);
            if constexpr (J == J_CB) { waitv<g2_cum(J) + g2_pieces(J) - (g2_cum(J_SB) + g2_pieces(J_SB) + 1)>(fs); flag_check(ws + 1024 * FS, fh, tag, fs); }
            if constexpr (J == J_SO) ldflag(fs, fo, ws);
            if constexpr (J == J_CO) {
                waitv<g2_cum(J) + g2_pieces(J) - (g2_cum(J_SO) + g2_pieces(J_SO) + 1)>(fs);
                flag_check(ws, fo, tag, fs);
            }
            if constexpr (J == J_SE) ldflag(fs, one, fe);
            if constexpr (J == J_CE) { waitv<0>(fs); flag_check(fe, one, tag, fs); }
            if constexpr (g2_next(J) >= 0) h_load(IC<g2_next(J)>{});
            SBAR();
        });
        __builtin_amdgcn_s_setprio(3);
        if (w != sp) {
#pragma unroll
            for (int g = 0; g < 9; ++g) {
                const f32x16& a = ac[g >> 1];
                const int o = 8 * (g & 1);
                auto r0 = __builtin_amdgcn_permlane32_swap(pack2(a[o], a[o + 1]), pack2(a[o + 4], a[o + 5]), false, false);
                auto r1 = __builtin_amdgcn_permlane32_swap(pack2(a[o + 2], a[o + 3]), pack2(a[o + 6], a[o + 7]), false, false);
                st16(u32x4{min(r0[0], EMPTY - 1), min(r1[0], EMPTY - 1), min(r0[1], EMPTY - 1), min(r1[1], EMPTY - 1)}, rs, vo16, WS_P + (dst * 4 + sp) * 9216 + g * 1024);
            }
        } else {
#pragma unroll
            for (int c = 0; c < 18; ++c) {
                const f32x16& a = ac[c >> 2];
                lds_st4(lds0 + G_Y + c * 1024 + (vo16 & 496) * 2 + (vo16 >> 9) * 16, a[4 * (c & 3)], a[4 * (c & 3) + 1], a[4 * (c & 3) + 2], a[4 * (c & 3) + 3]);
            }
            asm volatile("s_waitcnt lgkmcnt(0)" ::: "memory");
            cnt_post<C_P>(cb, one);
        }
    } else {
        const int a = w - 4, par = a & 1, kh = a >> 1, kh2 = a & 1;
        const bool own8 = (l31 >> 3) == a;
        u64 tag;
        __builtin_amdgcn_s_setprio(3);
        {
            u32x3 r[3];
            const unsigned vx = (2 * a + hi) * (DM * 2) + l31 * 12;
            const char* xs = x + (size_t)bid * 8 * DM * 2;
            asm volatile("global_load_dwordx3 %0, %4, %5\n\tglobal_load_dwordx3 %1, %4, %5 offset:384\n\tglobal_load_dwordx3 %2, %4, %5 offset:768\n\t"
                         "global_load_dwordx2 %3, %6, %7 sc1\n\ts_barrier\n\ts_waitcnt vmcnt(0)"
                         : "=&v"(r[0]), "=&v"(r[1]), "=&v"(r[2]), "=&v"(tag) : "v"(vx), "s"(xs), "v"(WS_EPOCH), "s"(ws) : "memory");
            float v[18], s = 0.f, q = 0.f;
#pragma unroll
            for (int i = 0; i < 9; ++i) { v[2 * i] = blo(r[i / 3][i % 3]); v[2 * i + 1] = bhi(r[i / 3][i % 3]); s += v[2 * i] + v[2 * i + 1]; }
            const float mu = half_sum(s) * (1.f / DM), mub = bf16r(mu);
#pragma unroll
            for (int i = 0; i < 18; ++i) q = fmaf(v[i] - mu, v[i] - mu, q);
            const float rstd = 1.f / bf16r(sqrtf(bf16r(bf16r(half_sum(q) * (1.f / DM)) + 1e-5f)));
            const unsigned dst = lds0 + L_XS + (2 * a + hi) * 1168 + l31 * 12;
#pragma unroll
            for (int i = 0; i < 9; ++i) {
                const unsigned d = pack2(v[2 * i] - mub, v[2 * i + 1] - mub);
                *(LDS_AS unsigned*)(size_t)(dst + (i / 3) * 384 + (i % 3) * 4) = pack2(blo(d) * rstd, bhi(d) * rstd);
            }
            tag = uni64(tag) + TAG_STEP;
            if (a == 0 && lane == 0) *(LDS_AS u64*)(size_t)(cb + C_TAG) = tag;
            asm volatile("s_waitcnt lgkmcnt(0)" ::: "memory");
            cnt_post<C_S>(cb, one);
            cnt_wait<C_S, 0>(cb, 4);
            if (a >= 2) {
#pragma unroll
                for (int i = 0; i < 5; ++i) {
                    const u32x4 d = *(const LDS_AS u32x4*)(size_t)(lds0 + L_XS + (lane & 7) * 1168 + ((4 * kh2 + i) * 8 + (lane >> 3)) * 16);
                    st16(d, rs, (lane >> 3) * 512 + (lane & 7) * 16, WS_XN + (bid >> 2) * 36864 + (bid & 3) * 128 + (4 * kh2 + i) * 4096);
                }
                cnt_post<C_C>(cb, one);
                waitn<0>();
                if (cnt_add(cb + C_B) == 1) flag_set(ws, WS_FX + bid * FS, tag, lane);
                q_groups(4 + 2 * kh2, 3);
                waitn<0>();
                cnt_post<C_F>(cb, one);
            } else {
                const unsigned ba = lds0 + L_KV + a * 27648 + (8 * hi + ((lane >> 2) & 3)) * 48 + ((lane & 16) ? 40 : 8 * (lane & 3));
                const unsigned xa = lds0 + L_XS + (a ? (l31 & 3) | ((l31 >> 1) & 4) : l31 & 7) * 1168 + hi * 16;
                f32x16 c = {};
                bf16x8 fw[5], fx[5];
                auto kv_read = [&](auto I) { fw[I.v % 5] = lds_tr2(ba + I.v * 768, ba + I.v * 768 + 192); fx[I.v % 5] = *(const LDS_AS bf16x8*)(size_t)(xa + I.v * 32); };
                cnt_wait<C_K, 0>(cb, 4);
                static_for<0, 4>(kv_read);
                if (a == 0) {
                    static_for<0, 36>([&](auto I) {
                        if constexpr (I.v + 4 < 36) kv_read(IC<I.v + 4>{});
                        SBAR(); c = MFMA32(fw[I.v % 5], fx[I.v % 5], c); SBAR();
                    });
                    PIN(c);
                    cnt_post<C_C>(cb, one);
                    const unsigned p0 = pack2(c[0], c[1]), p1 = pack2(c[2], c[3]), p2 = pack2(c[4], c[5]), p3 = pack2(c[6], c[7]), p4 = pack2(c[10], c[11]);
                    st16(u32x4{p0, p1, p2, p3}, rs, l31 < 8 ? hi * 512 + l31 * 16 : 0x80000000u, WS_K1 + (bid >> 2) * 1024 + (bid & 3) * 128);
                    __builtin_amdgcn_raw_buffer_store_b64(u32x2{p4, 0x3f80u}, rs, lane < 8 ? lane * 8 : 0x80000000u, WS_K2 + (bid >> 2) * 256 + (bid & 3) * 64, SC1);
                    float n2 = hi ? 0.f : blo(p4) * blo(p4) + bhi(p4) * bhi(p4);
                    n2 += blo(p0) * blo(p0) + bhi(p0) * bhi(p0) + blo(p1) * blo(p1) + bhi(p1) * bhi(p1);
                    n2 += blo(p2) * blo(p2) + bhi(p2) * bhi(p2) + blo(p3) * blo(p3) + bhi(p3) * bhi(p3);
                    const float kmax = half_max(sum32(n2));
                    waitn<0>();
                    flag_set(ws, WS_FKV + bid * 16, (u64)f2u(kmax) << 32 | (unsigned)tag, lane);
                } else {
                    static_for<0, 36>([&](auto I) {
                        if constexpr (I.v + 4 < 36) kv_read(IC<I.v + 4>{});
                        SBAR(); c = MFMA32(fx[I.v % 5], fw[I.v % 5], c); SBAR();
                    });
                    PIN(c);
                    cnt_post<C_C>(cb, one);
                    const u32x4 mv = {pack2(c[0], c[1]), pack2(c[2], c[3]), pack2(c[4], c[5]), pack2(c[6], c[7])};
                    st16(mv, rs, (lane < 16 || (lane & 62) == 18) ? (lane < 16 ? lane : lane - 2) * 16 : 0x80000000u, WS_V + bid * 288);
                    waitn<0>();
                    flag_set(ws, WS_FKV + bid * 16 + 8, tag, lane);
                }
            }
        }
        const f32x16 zero = {};
        bf16x8 q1[4];
        unsigned ql[4];
        float km;
        const char* kb = ws + WS_K1 + kh * 32 * 1024;
        const char* k2b = ws + WS_K2 + kh * 32 * 256;
        const char* vb = ws + WS_V + kh * 32 * 1152;
        unsigned vo8 = ((l31 >> 4) * 2 + ((l31 >> 2) & 1)) * 64 + (((l31 >> 3) & 1) * 4 + (l31 & 3)) * 8, vok = vo8 * 2 + hi * 512;
        unsigned vov = hi * 288 + l31 * 16;
        unsigned vo16m = own8 ? vo16 : 0x80000000u;
        PIN(vo8); PIN(vok); PIN(vov); PIN(vo16m);
        bf16x8 k1[4], v0[4], v1[4];
        s16x4 k2[4], q2[4];
#pragma unroll
        for (int i = 0; i < 4; ++i) { const unsigned cs = l31 == 18 ? 0x3f803f80u : 0u; v0[i] = v1[i] = __builtin_bit_cast(bf16x8, u32x4{cs, cs, cs, cs}); PIN(v0[i]); PIN(v1[i]); }
        {
            const char* fkv = ws + WS_FKV + kh * 2048;
            bf16x8 f0, f1;
            __builtin_amdgcn_s_setprio(0);
            cnt_wait<C_L, 8>(cb, KVW);
            __builtin_amdgcn_s_setprio(3);
            ld16<0>(f0, vo16, fkv); ld16<1024>(f1, vo16, fkv);
            for (unsigned lim = SPIN_MAX; lim; --lim) {
                waitv<0>(f0, f1);
                const u32x4 a0 = __builtin_bit_cast(u32x4, f0), a1 = __builtin_bit_cast(u32x4, f1);
                const unsigned tl = (unsigned)tag, tu = (unsigned)(tag >> 32);
                if (all_lanes(a0[0] == tl && a0[2] == tl && a0[3] == tu && a1[0] == tl && a1[2] == tl && a1[3] == tu)) break;
                __builtin_amdgcn_s_sleep(16);
                ld16<0>(f0, vo16, fkv); ld16<1024>(f1, vo16, fkv);
            }
            waitv<0>(f0, f1);
            km = fmaxf(u2f(__builtin_bit_cast(u32x4, f0)[1]), u2f(__builtin_bit_cast(u32x4, f1)[1]));
            ld16<0>(k1[0], vok, kb); ld8<0>(k2[0], vo8, k2b);
            static_for<0, 3>([&](auto B) {
                ld16<(B.v + 1) * 1024>(k1[B.v + 1], vok, kb); ld8<(B.v + 1) * 256>(k2[B.v + 1], vo8, k2b);
                ldv(v0[B.v], v1[B.v], vov, vb + B.v * 1152);
            });
            km = max32(half_max(km));
            PIN(km);
            __builtin_amdgcn_s_setprio(0);
#pragma unroll
            for (int r = 0; r < 3; ++r)
#pragma unroll
                for (int i = 0; i < 3; ++i) {
                    st16(u32x4{EMPTY, EMPTY, EMPTY, EMPTY}, rs, i < 2 ? vo16 : vo16m, WS_P + (bid * 4 + ((sp + 1 + r) & 3)) * 9216 + (i < 2 ? a + 4 * i : 8) * 1024);
                    asm volatile("s_sleep %0" :: "n"(ESLEEP) : "memory");
                }
            cnt_wait<C_L, 8>(cb, 40);
            __builtin_amdgcn_s_setprio(3);
            cnt_wait<C_Q, 1>(cb + 4 * par, 2);
#pragma unroll
            for (int qb = 0; qb < 4; ++qb) {
                const unsigned src = lds0 + L_Q + (4 * par + qb) * 1280;
                q1[qb] = *(const LDS_AS bf16x8*)(size_t)(src + vo16);
                ql[qb] = *(const LDS_AS unsigned*)(size_t)(src + 1024 + l31 * 4);
            }
            asm volatile("s_waitcnt lgkmcnt(0)" ::: "memory");
#pragma unroll
            for (int qb = 0; qb < 4; ++qb) { PIN(q1[qb]); PIN(ql[qb]); }
            cnt_post<C_A>(cb, one);
        }
        float mrow[4];
        bool safe = true;
#pragma unroll
        for (int qb = 0; qb < 4; ++qb) {
            const u32x4 u = __builtin_bit_cast(u32x4, q1[qb]);
            float n2 = hi ? 0.f : blo(ql[qb]) * blo(ql[qb]) + bhi(ql[qb]) * bhi(ql[qb]);
#pragma unroll
            for (int i = 0; i < 4; ++i) n2 += blo(u[i]) * blo(u[i]) + bhi(u[i]) * bhi(u[i]);
            const float bound = bf16r(__builtin_amdgcn_sqrtf(sum32(n2) * km) * 1.01f);
            safe = safe && (bound <= SM_SAFE);
            mrow[qb] = bound;
            q2[qb] = __builtin_bit_cast(s16x4, u32x2{hi ? 0u : ql[qb], hi ? 0u : pack2(-bound, 0.f)});
        }
        f32x16 o[4];
#pragma unroll
        for (int qb = 0; qb < 4; ++qb) o[qb] = f32x16{};
        asm volatile("s_waitcnt vmcnt(0)" : "+v"(k1[0]), "+v"(k1[1]), "+v"(k1[2]), "+v"(k1[3]), "+v"(k2[0]), "+v"(k2[1]), "+v"(k2[2]), "+v"(k2[3]),
                     "+v"(v0[0]), "+v"(v0[1]), "+v"(v0[2]), "+v"(v1[0]), "+v"(v1[1]), "+v"(v1[2]) :: "memory");
        __builtin_amdgcn_s_setprio(LOWT ? 0 : APRIO);
        if (all_lanes(safe)) {
            f32x16 s[2];
            unsigned p0[4], p1[4] = {0u, 0u, 0u, 0u};
            s[0] = MFMA32(k1[0], q1[0], zero); s[0] = MFMA8(k2[0], q2[0], s[0]);
            SBAR();
            for (int t4 = 0; t4 < 32; t4 += 4) {
                static_for<0, 16>([&](auto UU) {
                    constexpr int b = UU.v >> 2, qb = UU.v & 3, bn = (qb == 3) ? (b + 1) & 3 : b, bp = (qb == 0) ? (b + 3) & 3 : b;
                    f32x16& c = s[qb & 1];
                    f32x16& nx = s[(qb + 1) & 1];
                    float e0, e1, e2, e3;
#define EXP2(a, b, i) a = __builtin_amdgcn_exp2f(c[i]); b = __builtin_amdgcn_exp2f(c[i + 1]); PIN(a); PIN(b)
#define CVT2(p, i) p[i] = pack2(e0, e1); p[i + 1] = pack2(e2, e3); PIN(p[i]); PIN(p[i + 1])
                    if constexpr (LOWT && qb == 0 && b == (LOWT & 3)) { if (t4 == (LOWT & ~3)) __builtin_amdgcn_s_setprio(APRIO); }
                    EXP2(e0, e1, 0);
                    if constexpr (qb == 3) waitv<10>(k1[bn], k2[bn]);
                    SBAR();
                    nx = MFMA32(k1[bn], q1[(qb + 1) & 3], zero);
                    SBAR();
                    EXP2(e2, e3, 2); CVT2(p0, 0); EXP2(e0, e1, 4);
                    SBAR();
                    nx = MFMA8(k2[bn], q2[(qb + 1) & 3], nx);
                    SBAR();
                    if constexpr (qb == 3) {
                        const int tn = min(t4 + b + 4, 31);
                        ld16<0>(k1[b], vok, kb + tn * 1024); ld8<0>(k2[b], vo8, k2b + tn * 256);
                    }
                    EXP2(e2, e3, 6); CVT2(p0, 2); EXP2(e0, e1, 8);
                    SBAR();
                    o[(qb + 3) & 3] = MFMA32(v1[bp], __builtin_bit_cast(bf16x8, u32x4{p1[0], p1[1], p1[2], p1[3]}), o[(qb + 3) & 3]);
                    SBAR();
                    if constexpr (qb == 1) {
                        ldv(v0[(b + 3) & 3], v1[(b + 3) & 3], vov, vb + min(t4 + b + 3, 31) * 1152);
                    }
                    EXP2(e2, e3, 10); CVT2(p1, 0); EXP2(e0, e1, 12);
                    if constexpr (qb == 0) waitv<10>(v0[b], v1[b]);
                    SBAR();
                    o[qb] = MFMA32(v0[b], __builtin_bit_cast(bf16x8, u32x4{p0[0], p0[1], p0[2], p0[3]}), o[qb]);
                    SBAR();
                    EXP2(e2, e3, 14); CVT2(p1, 2);
                    SBAR();
                });
            }
            o[3] = MFMA32(v1[3], __builtin_bit_cast(bf16x8, u32x4{p1[0], p1[1], p1[2], p1[3]}), o[3]);
            asm volatile("s_waitcnt vmcnt(0)" : "+v"(k1[0]), "+v"(k1[1]), "+v"(k1[2]), "+v"(k1[3]), "+v"(k2[0]), "+v"(k2[1]), "+v"(k2[2]), "+v"(k2[3]),
                         "+v"(v0[0]), "+v"(v0[1]), "+v"(v0[2]), "+v"(v1[0]), "+v"(v1[1]), "+v"(v1[2]) :: "memory");
        } else {
#pragma unroll
            for (int qb = 0; qb < 4; ++qb) { q2[qb][2] = 0; mrow[qb] = -3.0e38f; }
#pragma unroll 1
            for (int t = 0; t < 32; ++t) {
                ld16<0>(k1[0], vok, kb + t * 1024); ld8<0>(k2[0], vo8, k2b + t * 256);
                ldv(v0[0], v1[0], vov, vb + t * 1152);
                waitv<0>(k1[0], k2[0]); waitv<0>(v0[0], v1[0]);
#pragma unroll
                for (int qb = 0; qb < 4; ++qb) {
                    f32x16 c = MFMA32(k1[0], q1[qb], zero);
                    c = MFMA8(k2[0], q2[qb], c);
                    float mx = c[0];
#pragma unroll
                    for (int r = 1; r < 16; ++r) mx = fmaxf(mx, c[r]);
                    const float mn = fmaxf(mrow[qb], max32(mx)), al = __builtin_amdgcn_exp2f(mrow[qb] - mn);
                    mrow[qb] = mn;
                    u32x4 p0, p1;
#pragma unroll
                    for (int r = 0; r < 4; ++r) {
                        p0[r] = pack2(__builtin_amdgcn_exp2f(c[2 * r] - mn), __builtin_amdgcn_exp2f(c[2 * r + 1] - mn));
                        p1[r] = pack2(__builtin_amdgcn_exp2f(c[8 + 2 * r] - mn), __builtin_amdgcn_exp2f(c[9 + 2 * r] - mn));
                    }
#pragma unroll
                    for (int r = 0; r < 16; ++r) o[qb][r] *= al;
                    o[qb] = MFMA32(v0[0], __builtin_bit_cast(bf16x8, p0), o[qb]);
                    o[qb] = MFMA32(v1[0], __builtin_bit_cast(bf16x8, p1), o[qb]);
                }
            }
        }
        __builtin_amdgcn_s_setprio(MPRIO);
        const unsigned ex = lds0 + L_EX + vo16;
#pragma unroll
        for (int j = 0; j < 2; ++j) {
            const f32x16& s = kh ? o[2 * j] : o[2 * j + 1];
            const float mm = kh ? mrow[2 * j] : mrow[2 * j + 1];
            lds_st4(ex + a * 6144 + j * 3072, s[0], s[1], s[2], s[3]);
            lds_st4(ex + a * 6144 + j * 3072 + 1024, s[4], s[5], s[6], s[7]);
            lds_st4(ex + a * 6144 + j * 3072 + 2048, s[8], s[9], s[10], mm);
        }
        asm volatile("s_waitcnt lgkmcnt(0)" ::: "memory");
        cnt_post<C_X>(cb + 4 * par, one);
        cnt_wait<C_X, 0>(cb + 4 * par, 2);
#pragma unroll
        for (int j = 0; j < 2; ++j) {
            const f32x16& s = kh ? o[2 * j + 1] : o[2 * j];
            const float ma = kh ? mrow[2 * j + 1] : mrow[2 * j];
            const unsigned src = ex + (a ^ 2) * 6144 + j * 3072;
            const f32x4 t0 = lds_f4(src), t1 = lds_f4(src + 1024), t2 = lds_f4(src + 2048);
            const float mb = t2[3], mm = fmaxf(ma, mb);
            const float fa = __builtin_amdgcn_exp2f(ma - mm), fb = __builtin_amdgcn_exp2f(mb - mm);
            const float inv = __builtin_amdgcn_rcpf(low32(s[10] * fa + t2[2] * fb)), ia = fa * inv, ib = fb * inv;
            const u32x4 pk = {pack2(s[0] * ia + t0[0] * ib, s[1] * ia + t0[1] * ib), pack2(s[2] * ia + t0[2] * ib, s[3] * ia + t0[3] * ib),
                              pack2(s[4] * ia + t1[0] * ib, s[5] * ia + t1[1] * ib), pack2(s[6] * ia + t1[2] * ib, s[7] * ia + t1[3] * ib)};
            const int tb = 8 * mt + 4 * par + 2 * j + kh;
            st16(pk, rs, vo16, WS_O + (tb * 36 + nt) * 1024);
            __builtin_amdgcn_raw_buffer_store_b32(pack2(s[8] * ia + t2[0] * ib, s[9] * ia + t2[1] * ib), rs, hi ? 0x80000000u : vo16,
                                                  WS_O + (tb * 36 + 32 + sp) * 1024 + (nt & 4) * 128 + (nt & 1) * 8 + (nt & 2) * 2, SC1);
        }
        waitn<0>();
        flag_set(ws, WS_FO + ((mt * 32 + nt) * 4 + a) * FS, tag, lane);
        if (cnt_add(cb + C_E) == 3) flag_set(ws, WS_FP + bid * FPS, tag, lane);
        __builtin_amdgcn_s_setprio(0);
        const size_t orow = (size_t)((4 * mt2 + sp) * 32 + l31) * (DM * 2) + nt2 * 288 + hi * 16;
        u32x4 xr[3];
#pragma unroll
        for (int i = 0; i < 3; ++i) xr[i] = *(const u32x4*)(x + orow + (i < 2 ? a + 4 * i : 8) * 32);
        if (bid == 0 && a == 1) {
#pragma unroll
            for (int r = 0; r < 4; ++r) flag_wait(ws, WS_FX + (r * 64 + lane) * FS, tag);
            flag_set(ws, WS_EPOCH, tag - TAG_STEP + 1, lane);
        }
        cnt_wait<C_P, 4>(cb, 1);
        f32x4 s0[3], s1[3];
#pragma unroll
        for (int i = 0; i < 3; ++i) {
            const unsigned src = lds0 + G_Y + (i < 2 ? a + 4 * i : 8) * 2048 + lane * 32;
            s0[i] = lds_f4(src) + f32x4{blo(xr[i][0]), bhi(xr[i][0]), blo(xr[i][1]), bhi(xr[i][1])};
            s1[i] = lds_f4(src + 16) + f32x4{blo(xr[i][2]), bhi(xr[i][2]), blo(xr[i][3]), bhi(xr[i][3])};
            PIN(s0[i]); PIN(s1[i]);
        }
        __builtin_amdgcn_s_sleep(PDELAY);
        u32x4 pp[3][3];
        for (unsigned lim = SPIN_MAX; lim; --lim) {
#pragma unroll
            for (int r = 0; r < 3; ++r)
#pragma unroll
                for (int i = 0; i < 3; ++i)
                    pp[r][i] = __builtin_amdgcn_raw_buffer_load_b128(rs, i < 2 ? vo16 : vo16m,
                                                                     WS_P + (bid * 4 + ((sp + 1 + r) & 3)) * 9216 + (i < 2 ? a + 4 * i : 8) * 1024, SC1);
            unsigned mx = 0;
#pragma unroll
            for (int r = 0; r < 3; ++r)
#pragma unroll
                for (int i = 0; i < 3; ++i) mx = max(max(mx, max(pp[r][i][0], pp[r][i][1])), max(pp[r][i][2], pp[r][i][3]));
            if (all_lanes(mx != EMPTY)) break;
            asm volatile("s_sleep %0" :: "n"(PSLEEP) : "memory");
        }
        SBAR();
#pragma unroll
        for (int i = 0; i < 3; ++i) {
#pragma unroll
            for (int r = 0; r < 3; ++r) {
                s0[i] += f32x4{blo(pp[r][i][0]), bhi(pp[r][i][0]), blo(pp[r][i][1]), bhi(pp[r][i][1])};
                s1[i] += f32x4{blo(pp[r][i][2]), bhi(pp[r][i][2]), blo(pp[r][i][3]), bhi(pp[r][i][3])};
            }
            if (i < 2 || own8)
                *(u32x4*)(out + orow + (i < 2 ? a + 4 * i : 8) * 32) = u32x4{pack2(s0[i][0], s0[i][1]), pack2(s0[i][2], s0[i][3]), pack2(s1[i][0], s1[i][1]), pack2(s1[i][2], s1[i][3])};
        }
    }
}

#ifdef __HIP_DEVICE_COMPILE__
#pragma clang attribute pop
#endif

extern "C" void kernel_launch(void* const* d_in, const int* in_sizes, int n_in, void* d_out, int out_size, void* d_ws, size_t ws_size,
                              hipStream_t stream) {
    (void)hipFuncSetAttribute((const void*)k_all, hipFuncAttributeMaxDynamicSharedMemorySize, LDS_BYTES);
    k_all<<<256, 512, LDS_BYTES, stream>>>((const char*)d_in[0], (const char*)d_in[1], (const char*)d_in[2], (const char*)d_in[3],
                                           (const char*)d_in[4], (const char*)d_in[6], (const char*)d_in[7], (const char*)d_in[8],
                                           (char*)d_ws, (char*)d_out);
}
```

```cpp
#include <hip/hip_runtime.h>
#include <stdint.h>

#ifdef __HIP_DEVICE_COMPILE__
#pragma clang attribute push(__attribute__((target("no-packed-fp32-ops"))), apply_to = function)
#endif

typedef __attribute__((ext_vector_type(8))) __bf16 bf16x8;
typedef __attribute__((ext_vector_type(2))) __bf16 bf16x2;
typedef __attribute__((ext_vector_type(4))) short s16x4;
typedef __attribute__((ext_vector_type(8))) short s16x8;
typedef __attribute__((ext_vector_type(4))) float f32x4;
typedef __attribute__((ext_vector_type(2))) float f32x2;
typedef __attribute__((ext_vector_type(16))) float f32x16;
typedef __attribute__((ext_vector_type(4))) unsigned u32x4;
typedef __attribute__((ext_vector_type(2))) unsigned u32x2;
typedef unsigned long long u64;
#define LDS_AS __attribute__((address_space(3)))
#define GLB_AS __attribute__((address_space(1)))

typedef __attribute__((ext_vector_type(3))) unsigned u32x3;

constexpr int DM = 576, FF = 4608;
constexpr unsigned FS = 64;
constexpr unsigned WS_EPOCH = 0, WS_FKV = 4096, WS_FX = 8192, WS_FO = WS_FX + 256 * FS, WS_FH = WS_FO + 1024 * FS, WS_FP = WS_FH + 2048 * FS;
constexpr int FPS = 128;
constexpr unsigned WS_XN = WS_FP + 1024 * 128;
constexpr unsigned WS_H = WS_XN + 64u * 36 * 1024;
constexpr unsigned WS_K1 = WS_H + 64u * 288 * 1024;
constexpr unsigned WS_K2 = WS_K1 + 64u * 1024;
constexpr unsigned WS_V = WS_K2 + 64u * 256;
constexpr unsigned WS_O = WS_V + 64u * 2048;
constexpr unsigned WS_P = WS_O + 64u * 36 * 1024;
constexpr unsigned WS_END = WS_P + 1024u * 9216;

constexpr int W_PART = 10368, W_SLOT = 2 * W_PART;
constexpr int L_KV = W_SLOT, L_WQ = L_KV + 2 * 27648;
constexpr int L_Q = L_WQ + 27648, L_XS = L_Q + 8 * 1280;
constexpr int G_X = 64512, G_Y = G_X + 36864, G_Z = 0, G_L = 36864;
constexpr int L_EX = G_Y + 36864;
constexpr int L_CNT = L_EX + 24576;
constexpr int C_L = 0, C_C = 4, C_A = 8, C_S = 12, C_F = 16, C_B = 20, C_Q = 28, C_X = 36, C_P = 44, C_K = 24, C_TAG = 56;
constexpr int LDS_BYTES = 163840;
static_assert(L_XS + 8 * 1168 <= L_CNT && 3 * W_SLOT <= G_X && L_CNT + 1024 <= LDS_BYTES && G_L + 9216 <= G_X, "lds map");

constexpr float SM_SAFE = 48.f;
constexpr int APRIO = 2;
constexpr unsigned SPIN_MAX = 1u << 21;
constexpr int GPRIO = 0;
constexpr int LOWT = 0;
constexpr int JSB = 12;
constexpr int KVW = 12;
constexpr int EPA = 2;
constexpr int CPF = 23;
constexpr int PFA = 0;
constexpr int PFB = 2;
constexpr int PFG = 9;
constexpr int MPRIO = 3;
constexpr u64 TAG_STEP = 0x9e3779b97f4a7c15ull;

#define SBAR() __builtin_amdgcn_sched_barrier(0)
#define MFMA32(a, b, c) __builtin_amdgcn_mfma_f32_32x32x16_bf16(a, b, c, 0, 0, 0)
#define MFMA8(a, b, c) __builtin_amdgcn_mfma_f32_32x32x8bf16_1k(a, b, c, 0, 0, 0)
#define PIN(x) asm volatile("" : "+v"(x))
#define SC1 16

__device__ __forceinline__ float u2f(unsigned u) { return __builtin_bit_cast(float, u); }
__device__ __forceinline__ unsigned f2u(float f) { return __builtin_bit_cast(unsigned, f); }
__device__ __forceinline__ bool all_lanes(bool p) { return __builtin_amdgcn_ballot_w64(!p) == 0; }
template <int N> struct IC { static constexpr int v = N; };
template <int I, int N, class F> __device__ __forceinline__ void static_for(F&& f) {
    if constexpr (I < N) { f(IC<I>{}); static_for<I + 1, N>(f); }
}

__device__ __forceinline__ void set_m0(unsigned lds_base) { asm volatile("s_mov_b32 m0, %0\n\ts_nop 0" :: "s"(lds_base) : "memory"); }
template <int OFF> __device__ __forceinline__ void glds(unsigned voff, const char* sbase) {
    asm volatile("global_load_lds_dwordx4 %0, %1 offset:%2" :: "v"(voff), "s"(sbase), "n"(OFF) : "memory");
}
template <int OFF> __device__ __forceinline__ void ld16(bf16x8& d, unsigned voff, const char* sbase) {
    asm volatile("global_load_dwordx4 %0, %1, %2 offset:%3 sc1" : "=v"(d) : "v"(voff), "s"(sbase), "n"(OFF) : "memory");
}
template <int OFF> __device__ __forceinline__ void ld8(s16x4& d, unsigned voff, const char* sbase) {
    asm volatile("global_load_dwordx2 %0, %1, %2 offset:%3 sc1" : "=v"(d) : "v"(voff), "s"(sbase), "n"(OFF) : "memory");
}
__device__ __forceinline__ void ldv(bf16x8& a, bf16x8& b, unsigned voff, const char* sbase) {
    asm volatile("s_mov_b64 exec, %4\n\tglobal_load_dwordx4 %0, %2, %3 sc1\n\tglobal_load_dwordx4 %1, %2, %3 offset:576 sc1\n\ts_mov_b64 exec, -1"
                 : "+v"(a), "+v"(b) : "v"(voff), "s"(sbase), "s"(0x0003ffff0003ffffull) : "memory");
}
__device__ __forceinline__ void touch(unsigned& d, unsigned voff, const char* sbase) {
    asm volatile("global_load_dword %0, %1, %2" : "+v"(d) : "v"(voff), "s"(sbase) : "memory");
}
__device__ __forceinline__ void ldflag(u64& d, unsigned voff, const char* sbase) {
    asm volatile("global_load_dwordx2 %0, %1, %2 sc1" : "=v"(d) : "v"(voff), "s"(sbase) : "memory");
}
template <int N, class T> __device__ __forceinline__ void waitv(T& a) { asm volatile("s_waitcnt vmcnt(%1)" : "+v"(a) : "n"(N < 63 ? N : 63) : "memory"); }
template <int N, class T, class U> __device__ __forceinline__ void waitv(T& a, U& b) {
    asm volatile("s_waitcnt vmcnt(%2)" : "+v"(a), "+v"(b) : "n"(N < 63 ? N : 63) : "memory");
}
template <class T> __device__ __forceinline__ void pin(T& a) { asm volatile("" : "+v"(a) :: "memory"); }
template <int N> __device__ __forceinline__ void waitn() { asm volatile("s_waitcnt vmcnt(%0)" :: "n"(N < 63 ? N : 63) : "memory"); }

__device__ __forceinline__ void st16(u32x4 v, __amdgpu_buffer_rsrc_t rs, unsigned voff, unsigned soff) {
    __builtin_amdgcn_raw_buffer_store_b128(v, rs, voff, soff, SC1);
    asm volatile("s_nop 1" :: "v"(v) : "memory");
}
__device__ __forceinline__ bf16x8 lds_tr2(unsigned a0, unsigned a1) {
    s16x4 lo = __builtin_amdgcn_ds_read_tr16_b64_v4i16((LDS_AS s16x4*)(size_t)a0);
    s16x4 hi = __builtin_amdgcn_ds_read_tr16_b64_v4i16((LDS_AS s16x4*)(size_t)a1);
    s16x8 r = __builtin_shufflevector(lo, hi, 0, 1, 2, 3, 4, 5, 6, 7);
    return __builtin_bit_cast(bf16x8, r);
}
__device__ __forceinline__ f32x4 lds_f4(unsigned a) { return *(const LDS_AS f32x4*)(size_t)a; }
__device__ __forceinline__ void lds_st4(unsigned a, float v0, float v1, float v2, float v3) { *(LDS_AS f32x4*)(size_t)a = f32x4{v0, v1, v2, v3}; }
__device__ __forceinline__ unsigned pack2(float lo, float hi) {
    bf16x2 v = {(__bf16)lo, (__bf16)hi};
    return __builtin_bit_cast(unsigned, v);
}
__device__ __forceinline__ float bf16r(float v) { return (float)(__bf16)v; }
__device__ __forceinline__ float blo(unsigned u) { return u2f(u << 16); }
__device__ __forceinline__ float bhi(unsigned u) { return u2f(u & 0xffff0000u); }
__device__ __forceinline__ float sum32(float a) {
    auto r = __builtin_amdgcn_permlane32_swap(f2u(a), f2u(a), false, false);
    return u2f(r[0]) + u2f(r[1]);
}
__device__ __forceinline__ float max32(float a) {
    auto r = __builtin_amdgcn_permlane32_swap(f2u(a), f2u(a), false, false);
    return fmaxf(u2f(r[0]), u2f(r[1]));
}
__device__ __forceinline__ float low32(float a) {
    auto r = __builtin_amdgcn_permlane32_swap(f2u(a), f2u(a), false, false);
    return u2f(r[0]);
}
__device__ __forceinline__ u64 uni64(u64 v) {
    return (u64)(unsigned)__builtin_amdgcn_readfirstlane((unsigned)(v >> 32)) << 32 | (unsigned)__builtin_amdgcn_readfirstlane((unsigned)v);
}
template <int CTRL> __device__ __forceinline__ float dpp(float a) { return u2f(__builtin_amdgcn_mov_dpp(f2u(a), CTRL, 0xf, 0xf, true)); }
__device__ __forceinline__ float half_sum(float a) {
    a += dpp<0xB1>(a); a += dpp<0x4E>(a); a += dpp<0x141>(a); a += dpp<0x140>(a);
    auto r = __builtin_amdgcn_permlane16_swap(f2u(a), f2u(a), false, false);
    return u2f(r[0]) + u2f(r[1]);
}
__device__ __forceinline__ float half_max(float a) {
    a = fmaxf(a, dpp<0xB1>(a)); a = fmaxf(a, dpp<0x4E>(a)); a = fmaxf(a, dpp<0x141>(a)); a = fmaxf(a, dpp<0x140>(a));
    auto r = __builtin_amdgcn_permlane16_swap(f2u(a), f2u(a), false, false);
    return fmaxf(u2f(r[0]), u2f(r[1]));
}
__device__ __forceinline__ unsigned cnt_add(unsigned addr) {
    unsigned old, r;
    asm volatile("s_mov_b64 exec, 1\n\tds_add_rtn_u32 %0, %2, %3\n\ts_waitcnt lgkmcnt(0)\n\tv_readfirstlane_b32 %1, %0\n\ts_mov_b64 exec, -1"
                 : "=&v"(old), "=s"(r) : "v"(addr), "v"(1u) : "memory");
    return r;
}
template <int OFF> __device__ __forceinline__ void cnt_post(unsigned base, unsigned one) {
    asm volatile("s_mov_b64 exec, 1\n\tds_add_u32 %0, %1 offset:%2\n\ts_mov_b64 exec, -1" :: "v"(base), "v"(one), "n"(OFF) : "memory");
}
template <int OFF, int SLEEP = 1> __device__ __forceinline__ void cnt_wait(unsigned base, unsigned target) {
    unsigned v, t, lim = SPIN_MAX * 8;
    asm volatile("1:\n\tds_read_b32 %0, %3 offset:%5\n\ts_waitcnt lgkmcnt(0)\n\tv_readfirstlane_b32 %1, %0\n\ts_cmp_ge_u32 %1, %4\n\ts_cbranch_scc1 2f\n\t"
                 "s_sleep %6\n\ts_sub_u32 %2, %2, 1\n\ts_cbranch_scc0 1b\n2:"
                 : "=&v"(v), "=&s"(t), "+s"(lim) : "v"(base), "s"(target), "n"(OFF), "n"(SLEEP) : "memory", "scc");
}
template <int OFF> __device__ __forceinline__ unsigned cnt_peek(unsigned base) { return *(volatile const LDS_AS unsigned*)(size_t)(base + OFF); }
template <int OFF, int TARGET> __device__ __forceinline__ void cnt_check(unsigned base, unsigned v) {
    unsigned t, lim;
    asm volatile("s_setprio 3\n\tv_readfirstlane_b32 %1, %0\n\ts_setprio %7\n\ts_cmp_ge_u32 %1, %4\n\ts_cbranch_scc1 2f\n\ts_mov_b32 %2, %6\n"
                 "1:\n\tds_read_b32 %0, %3 offset:%5\n\ts_waitcnt lgkmcnt(0)\n\tv_readfirstlane_b32 %1, %0\n\ts_cmp_ge_u32 %1, %4\n\ts_cbranch_scc1 2f\n\t"
                 "s_sub_u32 %2, %2, 1\n\ts_cbranch_scc0 1b\n2:"
                 : "+v"(v), "=&s"(t), "=&s"(lim) : "v"(base), "n"(TARGET), "n"(OFF), "n"(SPIN_MAX * 8), "n"(GPRIO) : "memory", "scc");
}
template <int OFF> __device__ __forceinline__ unsigned add_now(unsigned base) {
    unsigned r;
    asm volatile("s_setprio 3\n\tv_add_u32 %0, %2, %1\n\ts_setprio %3" : "=v"(r) : "v"(base), "n"(OFF), "n"(GPRIO));
    return r;
}
__device__ __forceinline__ void flag_set(char* ws, unsigned off, u64 tag, int lane) {
    if (lane == 0) __hip_atomic_store((GLB_AS u64*)(ws + off), tag, __ATOMIC_RELAXED, __HIP_MEMORY_SCOPE_AGENT);
}
__device__ __forceinline__ void flag_put(const char* ws, unsigned off, u64 tag) {
    unsigned o; u64 t;
    asm volatile("s_mov_b64 exec, 1\n\ts_setprio 3\n\tv_mov_b32 %0, %2\n\tv_mov_b64 %1, %3\n\ts_setprio %5\n\tglobal_store_dwordx2 %0, %1, %4 sc1\n\ts_mov_b64 exec, -1"
                 : "=&v"(o), "=&v"(t) : "s"(off), "s"(tag), "s"(ws), "n"(GPRIO) : "memory");
}
__device__ __forceinline__ void flag_wait(const char* ws, unsigned off, u64 tag) {
    u64 v; unsigned lim = SPIN_MAX;
    asm volatile("1:\n\tglobal_load_dwordx2 %0, %2, %3 sc1\n\ts_waitcnt vmcnt(0)\n\tv_cmp_ne_u64 vcc, %4, %0\n\ts_cmp_eq_u64 vcc, 0\n\ts_cbranch_scc1 2f\n\t"
                 "s_sleep 2\n\ts_sub_u32 %1, %1, 1\n\ts_cbranch_scc0 1b\n2:"
                 : "=&v"(v), "+s"(lim) : "v"(off), "s"(ws), "s"(tag) : "memory", "scc", "vcc");
}
__device__ __forceinline__ void flag_check(const char* ws, unsigned off, u64 tag, u64 v) {
    unsigned lim;
    asm volatile("s_setprio 3\n\tv_cmp_ne_u64 vcc, %4, %0\n\ts_setprio %6\n\ts_cmp_eq_u64 vcc, 0\n\ts_cbranch_scc1 2f\n\ts_mov_b32 %1, %5\n"
                 "1:\n\tglobal_load_dwordx2 %0, %2, %3 sc1\n\ts_waitcnt vmcnt(0)\n\tv_cmp_ne_u64 vcc, %4, %0\n\ts_cmp_eq_u64 vcc, 0\n\ts_cbranch_scc1 2f\n\t"
                 "s_sleep 1\n\ts_sub_u32 %1, %1, 1\n\ts_cbranch_scc0 1b\n2:"
                 : "+v"(v), "=&s"(lim) : "v"(off), "s"(ws), "s"(tag), "n"(SPIN_MAX), "n"(GPRIO) : "memory", "scc", "vcc");
}
__device__ __forceinline__ void swiglu4(unsigned& o0, unsigned& o1, float g0, float g1, float g2, float g3, float u0, float u1, float u2, float u3) {
    float t0, t1, t2, t3, p0, p1, p2, p3;
    asm("v_mul_f32 %2, 0xbfb8aa3b, %10\n\tv_mul_f32 %3, 0xbfb8aa3b, %11\n\tv_mul_f32 %4, 0xbfb8aa3b, %12\n\tv_mul_f32 %5, 0xbfb8aa3b, %13\n\t"
        "v_exp_f32 %2, %2\n\tv_exp_f32 %3, %3\n\tv_exp_f32 %4, %4\n\tv_exp_f32 %5, %5\n\t"
        "v_mul_f32 %6, %10, %14\n\tv_mul_f32 %7, %11, %15\n\tv_mul_f32 %8, %12, %16\n\tv_mul_f32 %9, %13, %17\n\t"
        "v_add_f32 %2, 1.0, %2\n\tv_add_f32 %3, 1.0, %3\n\tv_add_f32 %4, 1.0, %4\n\tv_add_f32 %5, 1.0, %5\n\t"
        "v_rcp_f32 %2, %2\n\tv_rcp_f32 %3, %3\n\tv_rcp_f32 %4, %4\n\tv_rcp_f32 %5, %5\n\t"
        "v_mul_f32 %6, %6, %2\n\tv_mul_f32 %7, %7, %3\n\tv_mul_f32 %8, %8, %4\n\tv_mul_f32 %9, %9, %5\n\t"
        "v_cvt_pk_bf16_f32 %0, %6, %7\n\tv_cvt_pk_bf16_f32 %1, %8, %9"
        : "=v"(o0), "=v"(o1), "=&v"(t0), "=&v"(t1), "=&v"(t2), "=&v"(t3), "=&v"(p0), "=&v"(p1), "=&v"(p2), "=&v"(p3)
        : "v"(g0), "v"(g1), "v"(g2), "v"(g3), "v"(u0), "v"(u1), "v"(u2), "v"(u3));
}

__device__ __forceinline__ int kperm(int r) { return (r & 8) + ((r & 1) << 2) + ((r >> 1) & 3); }

constexpr int S_FA = 42, S_SA = 60, S_CA = 66;
constexpr int g1_piece(int s, int u) { const int U = (s & 3) * 5 + u; return (U >= 2 && U <= ((s >> 2) <= 15 ? 6 : 10)) || (PFG && U == 13 && (s >> 2) >= PFG) ? 1 : 0; }
constexpr int g1_cum(int s, int u) {
    int n = 0;
    for (int t = 0; t <= s; ++t) {
        const int e = t < s ? 5 : u;
        if (e > 0 && t == S_FA) ++n;
        for (int i = 0; i < e; ++i) n += g1_piece(t, i);
        if (t < s) n += 2 + (t == S_SA ? 1 : 0) + (t == 35 ? 8 : 0);
    }
    return n;
}
constexpr int g1_upto_loads(int s) { return g1_cum(s + 1, 0) - (s == 35 ? 8 : 0); }
constexpr int g1_upto_unit(int T, int U) { return g1_cum(4 * T + U / 5, U % 5 + 1); }
constexpr int J_FB = 3, J_SB = JSB, J_CB = 16, J_SO = 58, J_CO = 63, HR = 16;
constexpr int G2_BASE = g1_cum(72, 0) + 10 + (HR - 12);
constexpr int g2_pieces(int J) { const int j = J & 7; return (J < 64 ? (j >= 1 && j <= 4 ? 2 : j == 5 ? 1 : 0) : 0) + (J == 6 ? 2 : 0); }
constexpr int g2_next(int J) { return J < 72 - HR ? J + HR : (J >= 64 && J <= 72) ? J + 8 : -1; }
constexpr int g2_misc(int J) { return (J == J_FB || J == J_SB || J == J_SO) ? 1 : 0; }
constexpr int g2_cum(int J) { int n = G2_BASE; for (int t = 0; t < J; ++t) n += g2_pieces(t) + g2_misc(t) + (g2_next(t) >= 0 ? 1 : 0); return n; }
constexpr int g2_upto_frag(int J) {
    if (J < 12) return g1_upto_loads(66 + J / 2) - ((J & 1) ? 0 : 1);
    if (J < HR) return G2_BASE - (HR - 1 - J);
    return g2_cum((J < 72 ? J - HR : J - 8) + 1);
}
constexpr int g2_slot(int T) { return T % 3 == 0 ? G_X : T % 3 == 1 ? G_Y : G_Z; }

__global__ __launch_bounds__(512, 2) void k_all(const char* __restrict__ x, const char* __restrict__ wq, const char* __restrict__ wk,
                                                const char* __restrict__ wv, const char* __restrict__ wo, const char* __restrict__ w1,
                                                const char* __restrict__ w3, const char* __restrict__ w2, char* ws, char* __restrict__ out) {
    extern __shared__ __attribute__((aligned(16))) char smem[];
    u64 pc;
    asm volatile("s_getpc_b64 %0" : "=s"(pc));
    const unsigned lds0 = (unsigned)(size_t)(LDS_AS char*)smem;
    const int lane = threadIdx.x & 63, w = __builtin_amdgcn_readfirstlane(threadIdx.x >> 6);
    const int bid = blockIdx.x;
    const int l31 = lane & 31, hi = lane >> 5;
    const __amdgpu_buffer_rsrc_t rs = __builtin_amdgcn_make_buffer_rsrc(ws, 0, WS_END, 0x00020000);
    const int th = bid & 1, sp = (bid >> 1) & 3, r_ = bid >> 3;
    const int mt = 4 * th + (r_ >> 3), nt = 8 * sp + (r_ & 7);
    const int mt2 = 8 * th + (r_ >> 2), nt2 = r_ & 3;
    unsigned pc0;
    if (CPF) {
        const unsigned vo = (r_ * CPF + min(lane, CPF - 1)) * 128;
        asm volatile("s_cmp_lg_u32 %3, 0\n\ts_cbranch_scc1 1f\n\tglobal_load_dword %0, %1, %2\n1:" : "=v"(pc0) : "v"(vo), "s"(pc), "s"(w) : "memory", "scc");
    }
    unsigned cb = lds0 + L_CNT, one = 1u, vo16 = lane * 16;
    PIN(cb); PIN(one); PIN(vo16);
    auto q_groups = [&](int g0, int n) {
        unsigned vi[3];
#pragma unroll
        for (int p = 0; p < 3; ++p) { const unsigned idx = 64 * p + lane, row = idx / 3, c = idx - row * 3; vi[p] = row * (DM * 2) + (c == 2 ? 20 : 16 * c) - p * 1024; }
#pragma unroll
        for (int g = 0; g < n; ++g) {
            const char* src = wq + nt * 36 + (g0 + g) * (64 * DM * 2);
            set_m0(lds0 + L_WQ + (g0 + g) * 3072);
            glds<0>(vi[0], src); glds<1024>(vi[1], src); glds<2048>(vi[2], src);
        }
    };
    if (w < 4) {
        if (w == 0 && lane < 14) *(LDS_AS unsigned*)(size_t)(cb + 4 * lane) = 0u;
        asm volatile("s_waitcnt lgkmcnt(0)\n\ts_barrier" ::: "memory");
        const char* wsrc = ((w >> 1) ? w3 : w1) + (size_t)(32 * (w & 1)) * (FF * 2);
        const char* wsrcA = wsrc + 128 * nt;
        const char* wsrcB = wsrc + 4096 + 160 * nt;
        const unsigned dstW = lds0 + (w >> 1) * W_PART + (w & 1) * 5120;
        unsigned voffW[9];
#pragma unroll
        for (int p = 0; p < 5; ++p) { const unsigned idx = 64 * p + lane, row = idx / 10; voffW[p] = row * (FF * 2) + ((idx - row * 10) & 7) * 16 - (p & 3) * 1024; }
        auto w_piece = [&](auto TT, auto PP) {
            constexpr int T = TT.v, p = PP.v;
            if constexpr ((p & 3) == 0) set_m0(dstW + (T % 3) * W_SLOT + p * 1024);
            glds<(p & 3) * 1024>(voffW[p], T < 9 ? wsrcA + (size_t)T * 64 * FF * 2 : wsrcB + (size_t)(T - 9) * 64 * FF * 2);
        };
        unsigned pf;
        {
            const unsigned idx = min(16 * w + lane, 63) % 54, row = idx / 3;
            const unsigned vo = (18 * r_ + row) * (DM * 2) + ((sp * 288) & ~127) + (idx - row * 3) * 128;
            asm volatile("s_mov_b64 exec, %3\n\tglobal_load_dword %0, %1, %2\n\ts_mov_b64 exec, -1" : "=v"(pf) : "v"(vo), "s"(wq), "s"(0xffffull) : "memory");
        }
        {
            const char* isrc = ((w >> 1) ? wv : wk) - 2048 + (w & 1) * (256 * 36);
            unsigned vi[3];
#pragma unroll
            for (int p = 0; p < 3; ++p) { const unsigned idx = 64 * p + lane, row = idx / 3, c = idx - row * 3; vi[p] = row * 36 + (c == 2 ? 20 : 16 * c) + 2048 - p * 1024; }
#pragma unroll
            for (int g = 0; g < 5; ++g) {
                set_m0(lds0 + L_KV + (w >> 1) * 27648 + ((w & 1) * 4 + g) * 3072);
                glds<0>(vi[0], isrc + g * 2304); glds<1024>(vi[1], isrc + g * 2304); glds<2048>(vi[2], isrc + g * 2304);
            }
        }
        static_for<0, 5>([&](auto P) { w_piece(IC<0>{}, P); });
        const unsigned wb = lds0 + ((lane >> 4) & 1) * W_PART + (8 * hi + ((lane >> 2) & 3)) * 160 + (lane & 3) * 8;
        f32x16 acc[5][2];
        q_groups(w, 1);
        const unsigned wqb = lds0 + L_WQ + (8 * hi + ((lane >> 2) & 3)) * 48 + ((lane & 16) ? 40 : 8 * (lane & 3));
        waitn<8>();
        cnt_post<C_K>(cb, one);
        if (PFA == 2) {
            const unsigned l = min(lane, 15);
            touch(pf, l < 7 ? (128 + 14 * r_ + 7 * (w & 1) + l) * (FF * 2) + 1024 * sp : (18 * r_ + 9 * (w & 1) + l - 7) * (FF * 2) + 4096 + 1280 * sp, (w >> 1) ? w3 : w1);
        }
        if (PFA == 3) {
            const unsigned idx = min((w & 1) * 64 + lane, 111);
            touch(pf, (128 + 14 * r_ + (idx >> 3)) * (FF * 2) + 1024 * sp + (idx & 7) * 128, (w >> 1) ? w3 : w1);
        }
        if (PFA == 1) {
            const unsigned idx = min(lane, 15);
            touch(pf, (128 + 4 * r_ + 2 * (w & 1) + (idx >> 3)) * (FF * 2) + 1024 * sp + (idx & 7) * 128, (w >> 1) ? w3 : w1);
        }
        cnt_wait<C_S>(cb, 4);
        const u64 tag = uni64(*(volatile const LDS_AS u64*)(size_t)(cb + C_TAG));
        flag_wait(ws, WS_FX + (mt * 32 + 8 * w + (lane & 7)) * FS, tag);
        PIN(pf);
        if (CPF) PIN(pc0);
        cnt_post<C_L>(cb, one);
        const int tb0 = mt * 8 + 2 * w;
        const char* xb = ws + WS_XN + (size_t)tb0 * 36864;
        unsigned vo16b = vo16 + 36864;
        PIN(vo16b);
        bf16x8 xr[6][2], wf[5], hr[HR];
        auto x_load = [&](auto SS) {
            constexpr int ks = SS.v % 36;
            ld16<(ks & 3) * 1024>(xr[SS.v % 6][0], vo16, xb + (ks >> 2) * 4096);
            ld16<(ks & 3) * 1024>(xr[SS.v % 6][1], vo16b, xb + (ks >> 2) * 4096);
        };
        static_for<0, 6>([&](auto S) { x_load(S); });
        cnt_wait<C_C>(cb, 4);
        static_for<0, 5>([&](auto P) { w_piece(IC<1>{}, P); });
        const int tbg = 4 * mt2 + w;
        const char* hbA = ws + WS_H + (size_t)(tbg * 288 + 32 * sp) * 1024;
        const char* hbB = ws + WS_H + (size_t)(tbg * 288 + 128 + 40 * sp) * 1024;
        const char* ob = ws + WS_O + (size_t)(tbg * 36) * 1024;
        auto h_load = [&](auto JJ) {
            constexpr int J = JJ.v, r = J < 32 ? J : J < 72 ? J - 32 : J - 72;
            if constexpr (J == 80) ld16<0>(hr[J % HR], vo16, ob + (32 + sp) * 1024);
            else ld16<(r & 3) * 1024>(hr[J % HR], vo16, (J < 32 ? hbA : J < 72 ? hbB : ob + sp * 8192) + (r >> 2) * 4096);
        };
        const char* w2A = w2 + ((size_t)(512 * sp + 32 * w) * DM + nt2 * 144) * 2;
        const char* w2B = w2 + ((size_t)(2048 + 640 * sp + 32 * w) * DM + nt2 * 144) * 2;
        const char* woS = wo + ((size_t)(18 * (8 * sp + 2 * w)) * DM + nt2 * 144) * 2;
        const unsigned dst2 = lds0 + w * 9216;
        auto w2_piece = [&](auto TT, auto PP) {
            constexpr int T = TT.v, p = PP.v, off = (p & 3) * 1024;
            if constexpr (off == 0) set_m0(dst2 + g2_slot(T) + p * 1024);
            if constexpr (T < 9) glds<off>(voffW[p], T < 4 ? w2A + (size_t)T * 128 * DM * 2 : w2B + (size_t)(T - 4) * 128 * DM * 2);
            else if constexpr (p == 4) {
                __builtin_amdgcn_s_setprio(3);
                SBAR();
                unsigned vo = voffW[p] + (vo16 >> 9) * (2 * DM * 2);
                PIN(vo);
                SBAR();
                __builtin_amdgcn_s_setprio(GPRIO);
                glds<off>(vo, woS);
            }
            else glds<off>(voffW[p], woS + (p > 4 ? 2 * DM * 2 : 0));
        };
        const unsigned fh = WS_FH + ((tbg >> 1) * 32 + 8 * sp + (lane & 7)) * FS;
        u64 fs;
        unsigned pk_c = 0, pk_l = 0;
        cnt_wait<C_L>(cb, 4);
        __builtin_amdgcn_s_setprio(GPRIO);
        const f32x16 zero = {};
        auto epilogue = [&](auto NBB, int ks0) {
            __builtin_amdgcn_s_setprio(NBB.v == 4 ? EPA : 3);
#pragma unroll
            for (int m = 0; m < 2; ++m)
#pragma unroll
                for (int u = 0; u < NBB.v; ++u) {
                    const f32x16& c = acc[u + 5 - NBB.v][m];
                    unsigned a0, a1, a2, a3;
                    swiglu4(a0, a1, c[0], c[1], c[2], c[3], c[8], c[9], c[10], c[11]);
                    swiglu4(a2, a3, c[4], c[5], c[6], c[7], c[12], c[13], c[14], c[15]);
                    st16(u32x4{a0, a1, a2, a3}, rs, vo16, WS_H + ((tb0 + m) * 288 + ks0 + u) * 1024);
                    SBAR();
                }
            __builtin_amdgcn_s_setprio(GPRIO);
        };
        static_for<0, 72>([&](auto SS) {
            constexpr int s = SS.v, NB = 5, NBUF = 5, T = s >> 2, sl = s % 36, UPS = 4 * NB;
            auto w_read = [&](auto GG) {
                constexpr int G = GG.v, st = (s / 36) * 36 + G / NB, off = ((st >> 2) % 3) * W_SLOT + (st & 3) * 2560 + (G % NB - (s < 36)) * 32;
                if constexpr (s < 36 && G % NB == 0) wf[G % NBUF] = lds_tr2(wqb + st * 768, wqb + st * 768 + 192);
                else wf[G % NBUF] = lds_tr2(wb + off, wb + off + 640);
            };
            if constexpr (sl == 0) { static_for<0, 4>([&](auto G) { w_read(G); }); SBAR(); }
            static_for<0, NB>([&](auto UU) {
                constexpr int u = UU.v, U = (s & 3) * NB + u, G = sl * NB + u;
                if constexpr (u == 0 && s < 6) waitv<g1_cum(s, 0) + 2 * (5 - s) + 5>(xr[s][0], xr[s][1]);
                if constexpr (u == 0 && s >= 6) {
                    if constexpr (s == S_CA) {
                        asm volatile("s_waitcnt vmcnt(%3)" : "+v"(xr[s % 6][0]), "+v"(xr[s % 6][1]), "+v"(fs) : "n"(g1_cum(s, 0) - g1_upto_loads(s - 6)) : "memory");
                        flag_check(ws, fh, tag, fs);
                    } else waitv<g1_cum(s, 0) - g1_upto_loads(s - 6)>(xr[s % 6][0], xr[s % 6][1]);
                    if constexpr (s == S_FA) flag_put(ws, WS_FH + ((mt * 4 + w) * 32 + nt) * FS, tag);
                }
                if constexpr (T < 17 && U == 12) {
                    waitn<g1_cum(s, u) - (T >= 1 ? g1_upto_unit(T - 1, 6) : 0)>();
                    cnt_post<C_L>(cb, one);
                }
                if constexpr (T == 17 && U == 15) { waitn<g1_cum(s, u) - g1_upto_unit(16, 10)>(); cnt_post<C_L>(cb, one); }
                if constexpr (U == 2) cnt_check<C_C, 4 * (T + 1)>(cb, pk_c);
                if constexpr (T == 3 && U == 3) cnt_wait<C_F>(cb, 2);
                if constexpr (T == 16 && U == 2) cnt_wait<C_A>(cb, 4);
                if constexpr (T < 17 && T != 8 && U == UPS - 4) cnt_check<C_L, 4 * (T + 2)>(cb, pk_l);
                if constexpr (U == 1) pk_c = cnt_peek<C_C>(cb);
                if constexpr (T < 17 && U == (T == 8 ? UPS - 1 : UPS - 5)) pk_l = cnt_peek<C_L>(cb);
                if constexpr (G + 4 < 36 * NB) w_read(IC<G + 4>{});
                if constexpr (T == 7 && U == 1) {
                    __builtin_amdgcn_s_setprio(3);
                    unsigned ln = vo16; PIN(ln); ln >>= 4;
#pragma unroll
                    for (int p = 0; p < 5; ++p) { const unsigned idx = 64 * p + ln; voffW[p] += (idx % 10 >= 8) ? 128 : 0; PIN(voffW[p]); }
                    __builtin_amdgcn_s_setprio(GPRIO);
                }
                if constexpr (T == 16 && U == 1) {
                    __builtin_amdgcn_s_setprio(3);
                    unsigned ln = vo16; PIN(ln); ln >>= 4;
#pragma unroll
                    for (int p = 0; p < 9; ++p) {
                        const unsigned idx = 64 * p + ln, r = idx / 18;
                        voffW[p] = ((r & 16) + kperm(r & 15)) * (DM * 2) + (idx - r * 18) * 16 - (p & 3) * 1024;
                        PIN(voffW[p]);
                    }
                    __builtin_amdgcn_s_setprio(GPRIO);
                }
                SBAR();
                acc[u][0] = MFMA32(wf[G % NBUF], xr[s % 6][0], sl == 0 ? zero : acc[u][0]);
                SBAR();
                if constexpr (U == UPS - 1) cnt_post<C_C>(cb, one);
                if constexpr (g1_piece(s, u)) {
                    if constexpr (U == 13) {
                        const unsigned b = min((T - PFG) * 128 + 4 * r_ + w, 1295) * 1024;
                        set_m0(lds0 + L_EX);
                        asm volatile("global_load_lds_dword %0, %1" :: "v"(vo16), "s"(w2 + b + (b < 512 * DM * 2 ? 512 * sp : 1536 + 640 * sp) * (DM * 2)) : "memory");
                    } else if constexpr (T <= 15) w_piece(IC<T + 2>{}, IC<U - 2>{});
                    else w2_piece(IC<T - 16>{}, IC<U - 2>{});
                }
                SBAR();
                acc[u][1] = MFMA32(wf[G % NBUF], xr[s % 6][1], sl == 0 ? zero : acc[u][1]);
                SBAR();
                if constexpr (s == 35 && u == 1) {
                    constexpr float QS = 0.23570226039551584f * 1.4426950408889634f;
                    __builtin_amdgcn_s_setprio(3);
#pragma unroll
                    for (int m = 0; m < 2; ++m) {
                        const f32x16& c = acc[0][m];
                        const unsigned dq = lds0 + L_Q + (2 * w + m) * 1280;
                        *(LDS_AS u32x4*)(size_t)(dq + vo16) = u32x4{pack2(c[0] * QS, c[1] * QS), pack2(c[2] * QS, c[3] * QS), pack2(c[4] * QS, c[5] * QS), pack2(c[6] * QS, c[7] * QS)};
                        *(LDS_AS unsigned*)(size_t)(dq + 1024 + (vo16 >> 2)) = pack2(c[10] * QS, c[11] * QS);
                    }
                    asm volatile("s_waitcnt lgkmcnt(0)" ::: "memory");
                    cnt_post<C_Q>(cb + 4 * (w >> 1), one);
                    __builtin_amdgcn_s_setprio(GPRIO);
                    SBAR();
                }
            });
            if constexpr (s == S_SA) ldflag(fs, fh, ws);
            if constexpr (s < 66) x_load(IC<s + 6>{});
            else { h_load(IC<2 * (s - 66)>{}); h_load(IC<2 * (s - 66) + 1>{}); }
            SBAR();
            if constexpr (s == 35) {
                epilogue(IC<4>{}, 4 * nt);
                cnt_check<C_L, 40>(cb, pk_l);
            }
        });
        __builtin_amdgcn_s_setprio(3);
        unsigned ln = vo16; PIN(ln); ln >>= 4;
        unsigned trl = lds0 + (2 * ((ln >> 2) & 3) + (ln >> 5)) * 288 + ((ln >> 4) & 1) * 32 + (ln & 3) * 8, ra = trl + G_X;
        unsigned fo = WS_FO + (((tbg >> 3) * 32 + 8 * sp + (ln & 7)) * 4 + ((tbg >> 2) & 1) + 2 * (tbg & 1)) * FS;
        PIN(trl); PIN(ra); PIN(fo);
        cnt_wait<C_L>(cb, 76);
        epilogue(IC<5>{}, 128 + 5 * nt);
        static_for<12, HR>([&](auto J) { h_load(J); });
        f32x16 ac[5];
        bf16x8 vf[2][5];
#pragma unroll
        for (int n = 0; n < 5; ++n) vf[0][n] = lds_tr2(ra + n * 64, ra + n * 64 + 2304);
        static_for<0, 81>([&](auto JJ) {
            constexpr int J = JJ.v, T = J >> 3, j = J & 7;
            if constexpr (j == 5 && T < 9) {
                waitn<g2_cum(J) - (T == 0 ? g1_upto_unit(17, 10) : g2_cum(J - 8) + g2_pieces(J - 8))>();
                cnt_post<C_L>(cb, one);
            }
            if constexpr (j == 1 && T < 9) cnt_check<C_C, 4 * (19 + T)>(cb, pk_c);
            if constexpr (j == 7 && T < 9) {
                asm volatile("s_waitcnt lgkmcnt(0)" ::: "memory");
                if constexpr (T < 8) cnt_post<C_C>(cb, one);
                cnt_check<C_L, 4 * (20 + T)>(cb, pk_l);
                ra = add_now<g2_slot(T + 1)>(trl);
            }
            if constexpr (J == 79) ra = add_now<G_L>(trl);
            waitv<g2_cum(J) - g2_upto_frag(J)>(hr[J % HR]);
            if constexpr (j == 0 && T < 9) pk_c = cnt_peek<C_C>(cb);
            if constexpr (j == 6 && T < 9) pk_l = cnt_peek<C_L>(cb);
            SBAR();
            static_for<0, 5>([&](auto NN) {
                constexpr int n = NN.v, nx = ((J + 1) & 7) * 4608 + n * 64;
                if constexpr (J < 80) vf[(J + 1) & 1][n] = lds_tr2(ra + nx, ra + nx + 2304);
                SBAR();
                ac[n] = MFMA32(vf[J & 1][n], hr[J % HR], J == 0 ? zero : ac[n]);
                SBAR();
                if constexpr (J < 64 && j >= 1 && j <= 5 && (n == 1 || (n == 3 && j < 5))) w2_piece(IC<T + 2>{}, IC<2 * (j - 1) + n / 2>{});
                if constexpr (J == 6 && (n == 1 || n == 3)) {
                    __builtin_amdgcn_s_setprio(3);
                    SBAR();
                    unsigned ln = vo16; PIN(ln); ln >>= 4;
                    const unsigned pc = n == 1 ? w : 4, idx = min(64 * pc + ln, 287u), r = idx / 18, a_ = r >> 3, b_ = r & 1, c_ = (r >> 1) & 3;
                    unsigned vo = (18 * (4 * b_ + a_ + (c_ & 2)) + 16 + (c_ & 1)) * (DM * 2) + (idx - r * 18) * 16;
                    PIN(vo);
                    SBAR();
                    __builtin_amdgcn_s_setprio(GPRIO);
                    set_m0(lds0 + G_L + pc * 1024);
                    glds<0>(vo, wo + ((size_t)(144 * sp) * DM + nt2 * 144) * 2);
                }
                SBAR();
            });
            if constexpr (J == J_FB) {
                waitn<g2_cum(J) + g2_pieces(J) - (g1_cum(72, 0) + 10)>();
                flag_put(ws, WS_FH + ((32 + mt * 4 + w) * 32 + nt) * FS, tag);
            }
            if constexpr (J == J_SB) ldflag(fs, fh, ws + 1024 * FS);
            if constexpr (J == J_CB) { waitv<g2_cum(J) + g2_pieces(J) - (g2_cum(J_SB) + g2_pieces(J_SB) + 1)>(fs); flag_check(ws + 1024 * FS, fh, tag, fs); }
            if constexpr (J == J_SO) ldflag(fs, fo, ws);
            if constexpr (J == J_CO) {
                waitv<g2_cum(J) + g2_pieces(J) - (g2_cum(J_SO) + g2_pieces(J_SO) + 1)>(fs);
                flag_check(ws, fo, tag, fs);
            }
            if constexpr (g2_next(J) >= 0) h_load(IC<g2_next(J)>{});
            SBAR();
        });
        __builtin_amdgcn_s_setprio(3);
        if (w != sp) {
            const unsigned dst = ((bid & ~6) | (w << 1)) * 4 + sp;
#pragma unroll
            for (int g = 0; g < 9; ++g) {
                const f32x16& a = ac[g >> 1];
                const int o = 8 * (g & 1);
                auto r0 = __builtin_amdgcn_permlane32_swap(pack2(a[o], a[o + 1]), pack2(a[o + 4], a[o + 5]), false, false);
                auto r1 = __builtin_amdgcn_permlane32_swap(pack2(a[o + 2], a[o + 3]), pack2(a[o + 6], a[o + 7]), false, false);
                st16(u32x4{r0[0], r1[0], r0[1], r1[1]}, rs, vo16, WS_P + dst * 9216 + g * 1024);
            }
            waitn<0>();
            flag_set(ws, WS_FP + dst * FPS, tag, vo16);
        } else {
#pragma unroll
            for (int c = 0; c < 18; ++c) {
                const f32x16& a = ac[c >> 2];
                lds_st4(lds0 + G_Y + c * 1024 + (vo16 & 496) * 2 + (vo16 >> 9) * 16, a[4 * (c & 3)], a[4 * (c & 3) + 1], a[4 * (c & 3) + 2], a[4 * (c & 3) + 3]);
            }
            asm volatile("s_waitcnt lgkmcnt(0)" ::: "memory");
            cnt_post<C_P>(cb, one);
        }
    } else {
        const int a = w - 4, par = a & 1, kh = a >> 1, kh2 = a & 1;
        const bool own8 = (l31 >> 3) == a;
        u64 tag;
        __builtin_amdgcn_s_setprio(3);
        {
            u32x3 r[3];
            const unsigned vx = (2 * a + hi) * (DM * 2) + l31 * 12;
            const char* xs = x + (size_t)bid * 8 * DM * 2;
            asm volatile("global_load_dwordx3 %0, %4, %5\n\tglobal_load_dwordx3 %1, %4, %5 offset:384\n\tglobal_load_dwordx3 %2, %4, %5 offset:768\n\t"
                         "global_load_dwordx2 %3, %6, %7 sc1\n\ts_barrier\n\ts_waitcnt vmcnt(0)"
                         : "=&v"(r[0]), "=&v"(r[1]), "=&v"(r[2]), "=&v"(tag) : "v"(vx), "s"(xs), "v"(WS_EPOCH), "s"(ws) : "memory");
            float v[18], s = 0.f, q = 0.f;
#pragma unroll
            for (int i = 0; i < 9; ++i) { v[2 * i] = blo(r[i / 3][i % 3]); v[2 * i + 1] = bhi(r[i / 3][i % 3]); s += v[2 * i] + v[2 * i + 1]; }
            const float mu = half_sum(s) * (1.f / DM), mub = bf16r(mu);
#pragma unroll
            for (int i = 0; i < 18; ++i) q = fmaf(v[i] - mu, v[i] - mu, q);
            const float rstd = 1.f / bf16r(sqrtf(bf16r(bf16r(half_sum(q) * (1.f / DM)) + 1e-5f)));
            const unsigned dst = lds0 + L_XS + (2 * a + hi) * 1168 + l31 * 12;
#pragma unroll
            for (int i = 0; i < 9; ++i) {
                const unsigned d = pack2(v[2 * i] - mub, v[2 * i + 1] - mub);
                *(LDS_AS unsigned*)(size_t)(dst + (i / 3) * 384 + (i % 3) * 4) = pack2(blo(d) * rstd, bhi(d) * rstd);
            }
            tag = uni64(tag) + TAG_STEP;
            if (a == 0 && lane == 0) *(LDS_AS u64*)(size_t)(cb + C_TAG) = tag;
            asm volatile("s_waitcnt lgkmcnt(0)" ::: "memory");
            cnt_post<C_S>(cb, one);
            cnt_wait<C_S, 0>(cb, 4);
            if (a >= 2) {
#pragma unroll
                for (int i = 0; i < 5; ++i) {
                    const u32x4 d = *(const LDS_AS u32x4*)(size_t)(lds0 + L_XS + (lane & 7) * 1168 + ((4 * kh2 + i) * 8 + (lane >> 3)) * 16);
                    st16(d, rs, (lane >> 3) * 512 + (lane & 7) * 16, WS_XN + (bid >> 2) * 36864 + (bid & 3) * 128 + (4 * kh2 + i) * 4096);
                }
                cnt_post<C_C>(cb, one);
                waitn<0>();
                if (cnt_add(cb + C_B) == 1) flag_set(ws, WS_FX + bid * FS, tag, lane);
                q_groups(4 + 2 * kh2, 3);
                waitn<0>();
                cnt_post<C_F>(cb, one);
            } else {
                const unsigned ba = lds0 + L_KV + a * 27648 + (8 * hi + ((lane >> 2) & 3)) * 48 + ((lane & 16) ? 40 : 8 * (lane & 3));
                const unsigned xa = lds0 + L_XS + (a ? (l31 & 3) | ((l31 >> 1) & 4) : l31 & 7) * 1168 + hi * 16;
                f32x16 c = {};
                bf16x8 fw[5], fx[5];
                auto kv_read = [&](auto I) { fw[I.v % 5] = lds_tr2(ba + I.v * 768, ba + I.v * 768 + 192); fx[I.v % 5] = *(const LDS_AS bf16x8*)(size_t)(xa + I.v * 32); };
                cnt_wait<C_K, 0>(cb, 4);
                static_for<0, 4>(kv_read);
                if (a == 0) {
                    static_for<0, 36>([&](auto I) {
                        if constexpr (I.v + 4 < 36) kv_read(IC<I.v + 4>{});
                        SBAR(); c = MFMA32(fw[I.v % 5], fx[I.v % 5], c); SBAR();
                    });
                    PIN(c);
                    cnt_post<C_C>(cb, one);
                    const unsigned p0 = pack2(c[0], c[1]), p1 = pack2(c[2], c[3]), p2 = pack2(c[4], c[5]), p3 = pack2(c[6], c[7]), p4 = pack2(c[10], c[11]);
                    st16(u32x4{p0, p1, p2, p3}, rs, l31 < 8 ? hi * 512 + l31 * 16 : 0x80000000u, WS_K1 + (bid >> 2) * 1024 + (bid & 3) * 128);
                    __builtin_amdgcn_raw_buffer_store_b64(u32x2{p4, 0x3f80u}, rs, lane < 8 ? lane * 8 : 0x80000000u, WS_K2 + (bid >> 2) * 256 + (bid & 3) * 64, SC1);
                    float n2 = hi ? 0.f : blo(p4) * blo(p4) + bhi(p4) * bhi(p4);
                    n2 += blo(p0) * blo(p0) + bhi(p0) * bhi(p0) + blo(p1) * blo(p1) + bhi(p1) * bhi(p1);
                    n2 += blo(p2) * blo(p2) + bhi(p2) * bhi(p2) + blo(p3) * blo(p3) + bhi(p3) * bhi(p3);
                    const float kmax = half_max(sum32(n2));
                    waitn<0>();
                    flag_set(ws, WS_FKV + bid * 16, (u64)f2u(kmax) << 32 | (unsigned)tag, lane);
                } else {
                    static_for<0, 36>([&](auto I) {
                        if constexpr (I.v + 4 < 36) kv_read(IC<I.v + 4>{});
                        SBAR(); c = MFMA32(fx[I.v % 5], fw[I.v % 5], c); SBAR();
                    });
                    PIN(c);
                    cnt_post<C_C>(cb, one);
                    const u32x4 mv = {pack2(c[0], c[1]), pack2(c[2], c[3]), pack2(c[4], c[5]), pack2(c[6], c[7])};
                    st16(mv, rs, (lane < 16 || (lane & 62) == 18) ? (lane < 16 ? lane : lane - 2) * 16 : 0x80000000u, WS_V + bid * 288);
                    waitn<0>();
                    flag_set(ws, WS_FKV + bid * 16 + 8, tag, lane);
                }
            }
        }
        const f32x16 zero = {};
        bf16x8 q1[4];
        unsigned ql[4];
        float km;
        const char* kb = ws + WS_K1 + kh * 32 * 1024;
        const char* k2b = ws + WS_K2 + kh * 32 * 256;
        const char* vb = ws + WS_V + kh * 32 * 1152;
        unsigned vo8 = ((l31 >> 4) * 2 + ((l31 >> 2) & 1)) * 64 + (((l31 >> 3) & 1) * 4 + (l31 & 3)) * 8, vok = vo8 * 2 + hi * 512;
        unsigned vov = hi * 288 + l31 * 16;
        PIN(vo8); PIN(vok); PIN(vov);
        bf16x8 k1[4], v0[4], v1[4];
        s16x4 k2[4], q2[4];
#pragma unroll
        for (int i = 0; i < 4; ++i) { const unsigned cs = l31 == 18 ? 0x3f803f80u : 0u; v0[i] = v1[i] = __builtin_bit_cast(bf16x8, u32x4{cs, cs, cs, cs}); PIN(v0[i]); PIN(v1[i]); }
        {
            const char* fkv = ws + WS_FKV + kh * 2048;
            bf16x8 f0, f1;
            unsigned pf = 0;
            __builtin_amdgcn_s_setprio(0);
            const unsigned l17 = min(lane, 17);
            const unsigned vt1 = (2 * r_ + (a & 1)) * (FF * 2) + (l17 < 10 ? 4096 + 1280 * sp + l17 * 128 : 256 * FF * 2 + 1024 * sp + (l17 - 10) * 128);
            const unsigned vt1b = (2 * r_ + (a & 1)) * (FF * 2) + 4096 + 1280 * sp + min(lane, 9) * 128;
            const unsigned vt1a = (2 * r_ + (a & 1)) * (FF * 2) + 1024 * sp + min(lane, 7) * 128;
            const unsigned vt2 = (4 * r_ + a) * (DM * 2) + min(lane, 8) * 128;
            auto side = [&](int i) {
                cnt_wait<C_L, 8>(cb, 4 * (i + 1));
                if (PFB == 4 && i == 0) {
                    const unsigned idx = min((a & 1) * 64 + lane, 111);
                    touch(pf, (128 + 14 * r_ + (idx >> 3)) * (FF * 2) + 1024 * sp + (idx & 7) * 128, (a >> 1) ? w3 : w1);
                }
                if (PFB == 5) touch(pf, vt1b, ((a >> 1) ? w3 : w1) + (size_t)i * 64 * FF * 2);
                if (PFB == 4 && i >= 2) touch(pf, vt1b, ((a >> 1) ? w3 : w1) + (size_t)(i - 2) * 64 * FF * 2);
                if (PFB == 1) touch(pf, i < 5 ? vt1 : vt1b, ((a >> 1) ? w3 : w1) + (size_t)i * 64 * FF * 2);
                if ((PFB == 2 || PFB == 3) && i < 5) touch(pf, vt1a, ((a >> 1) ? w3 : w1) + (size_t)(i + 4) * 64 * FF * 2);
                if (PFB == 2 && i >= 4) touch(pf, vt1b, ((a >> 1) ? w3 : w1) + (size_t)(i - 4) * 64 * FF * 2);
            };
#pragma unroll
            for (int i = 0; i < KVW / 4 - 1; ++i) side(i);
            cnt_wait<C_L, 8>(cb, KVW);
            __builtin_amdgcn_s_setprio(3);
            ld16<0>(f0, vo16, fkv); ld16<1024>(f1, vo16, fkv);
            for (unsigned lim = SPIN_MAX; lim; --lim) {
                waitv<0>(f0, f1);
                const u32x4 a0 = __builtin_bit_cast(u32x4, f0), a1 = __builtin_bit_cast(u32x4, f1);
                const unsigned tl = (unsigned)tag, tu = (unsigned)(tag >> 32);
                if (all_lanes(a0[0] == tl && a0[2] == tl && a0[3] == tu && a1[0] == tl && a1[2] == tl && a1[3] == tu)) break;
                __builtin_amdgcn_s_sleep(16);
                ld16<0>(f0, vo16, fkv); ld16<1024>(f1, vo16, fkv);
            }
            waitv<0>(f0, f1);
            km = fmaxf(u2f(__builtin_bit_cast(u32x4, f0)[1]), u2f(__builtin_bit_cast(u32x4, f1)[1]));
            ld16<0>(k1[0], vok, kb); ld8<0>(k2[0], vo8, k2b);
            static_for<0, 3>([&](auto B) {
                ld16<(B.v + 1) * 1024>(k1[B.v + 1], vok, kb); ld8<(B.v + 1) * 256>(k2[B.v + 1], vo8, k2b);
                ldv(v0[B.v], v1[B.v], vov, vb + B.v * 1152);
            });
            km = max32(half_max(km));
            PIN(km);
            __builtin_amdgcn_s_setprio(0);
#pragma unroll
            for (int i = KVW / 4 - 1; i < 9; ++i) side(i);
            if (PFG) touch(pf, l17 < 9 ? vt2 : (128 + ((4 * r_ + a) & 15)) * (DM * 2) + (l17 - 9) * 128, wo + (size_t)144 * sp * (DM * 2));
            cnt_wait<C_L, 8>(cb, 40);
            __builtin_amdgcn_s_setprio(3);
            cnt_wait<C_Q, 1>(cb + 4 * par, 2);
#pragma unroll
            for (int qb = 0; qb < 4; ++qb) {
                const unsigned src = lds0 + L_Q + (4 * par + qb) * 1280;
                q1[qb] = *(const LDS_AS bf16x8*)(size_t)(src + vo16);
                ql[qb] = *(const LDS_AS unsigned*)(size_t)(src + 1024 + l31 * 4);
            }
            asm volatile("s_waitcnt lgkmcnt(0)" ::: "memory");
#pragma unroll
            for (int qb = 0; qb < 4; ++qb) { PIN(q1[qb]); PIN(ql[qb]); }
            cnt_post<C_A>(cb, one);
            waitv<0>(pf);
        }
        float mrow[4];
        bool safe = true;
#pragma unroll
        for (int qb = 0; qb < 4; ++qb) {
            const u32x4 u = __builtin_bit_cast(u32x4, q1[qb]);
            float n2 = hi ? 0.f : blo(ql[qb]) * blo(ql[qb]) + bhi(ql[qb]) * bhi(ql[qb]);
#pragma unroll
            for (int i = 0; i < 4; ++i) n2 += blo(u[i]) * blo(u[i]) + bhi(u[i]) * bhi(u[i]);
            const float bound = bf16r(__builtin_amdgcn_sqrtf(sum32(n2) * km) * 1.01f);
            safe = safe && (bound <= SM_SAFE);
            mrow[qb] = bound;
            q2[qb] = __builtin_bit_cast(s16x4, u32x2{hi ? 0u : ql[qb], hi ? 0u : pack2(-bound, 0.f)});
        }
        f32x16 o[4];
#pragma unroll
        for (int qb = 0; qb < 4; ++qb) o[qb] = f32x16{};
        asm volatile("s_waitcnt vmcnt(0)" : "+v"(k1[0]), "+v"(k1[1]), "+v"(k1[2]), "+v"(k1[3]), "+v"(k2[0]), "+v"(k2[1]), "+v"(k2[2]), "+v"(k2[3]),
                     "+v"(v0[0]), "+v"(v0[1]), "+v"(v0[2]), "+v"(v1[0]), "+v"(v1[1]), "+v"(v1[2]) :: "memory");
        __builtin_amdgcn_s_setprio(LOWT ? 0 : APRIO);
        if (all_lanes(safe)) {
            f32x16 s[2];
            unsigned p0[4], p1[4] = {0u, 0u, 0u, 0u};
            s[0] = MFMA32(k1[0], q1[0], zero); s[0] = MFMA8(k2[0], q2[0], s[0]);
            SBAR();
            for (int t4 = 0; t4 < 32; t4 += 4) {
                static_for<0, 16>([&](auto UU) {
                    constexpr int b = UU.v >> 2, qb = UU.v & 3, bn = (qb == 3) ? (b + 1) & 3 : b, bp = (qb == 0) ? (b + 3) & 3 : b;
                    f32x16& c = s[qb & 1];
                    f32x16& nx = s[(qb + 1) & 1];
                    float e0, e1, e2, e3;
#define EXP2(a, b, i) a = __builtin_amdgcn_exp2f(c[i]); b = __builtin_amdgcn_exp2f(c[i + 1]); PIN(a); PIN(b)
#define CVT2(p, i) p[i] = pack2(e0, e1); p[i + 1] = pack2(e2, e3); PIN(p[i]); PIN(p[i + 1])
                    if constexpr (LOWT && qb == 0 && b == (LOWT & 3)) { if (t4 == (LOWT & ~3)) __builtin_amdgcn_s_setprio(APRIO); }
                    EXP2(e0, e1, 0);
                    if constexpr (qb == 3) waitv<10>(k1[bn], k2[bn]);
                    SBAR();
                    nx = MFMA32(k1[bn], q1[(qb + 1) & 3], zero);
                    SBAR();
                    EXP2(e2, e3, 2); CVT2(p0, 0); EXP2(e0, e1, 4);
                    SBAR();
                    nx = MFMA8(k2[bn], q2[(qb + 1) & 3], nx);
                    SBAR();
                    if constexpr (qb == 3) {
                        const int tn = min(t4 + b + 4, 31);
                        ld16<0>(k1[b], vok, kb + tn * 1024); ld8<0>(k2[b], vo8, k2b + tn * 256);
                    }
                    EXP2(e2, e3, 6); CVT2(p0, 2); EXP2(e0, e1, 8);
                    SBAR();
                    o[(qb + 3) & 3] = MFMA32(v1[bp], __builtin_bit_cast(bf16x8, u32x4{p1[0], p1[1], p1[2], p1[3]}), o[(qb + 3) & 3]);
                    SBAR();
                    if constexpr (qb == 1) {
                        ldv(v0[(b + 3) & 3], v1[(b + 3) & 3], vov, vb + min(t4 + b + 3, 31) * 1152);
                    }
                    EXP2(e2, e3, 10); CVT2(p1, 0); EXP2(e0, e1, 12);
                    if constexpr (qb == 0) waitv<10>(v0[b], v1[b]);
                    SBAR();
                    o[qb] = MFMA32(v0[b], __builtin_bit_cast(bf16x8, u32x4{p0[0], p0[1], p0[2], p0[3]}), o[qb]);
                    SBAR();
                    EXP2(e2, e3, 14); CVT2(p1, 2);
                    SBAR();
                });
            }
            o[3] = MFMA32(v1[3], __builtin_bit_cast(bf16x8, u32x4{p1[0], p1[1], p1[2], p1[3]}), o[3]);
            asm volatile("s_waitcnt vmcnt(0)" : "+v"(k1[0]), "+v"(k1[1]), "+v"(k1[2]), "+v"(k1[3]), "+v"(k2[0]), "+v"(k2[1]), "+v"(k2[2]), "+v"(k2[3]),
                         "+v"(v0[0]), "+v"(v0[1]), "+v"(v0[2]), "+v"(v1[0]), "+v"(v1[1]), "+v"(v1[2]) :: "memory");
        } else {
#pragma unroll
            for (int qb = 0; qb < 4; ++qb) { q2[qb][2] = 0; mrow[qb] = -3.0e38f; }
#pragma unroll 1
            for (int t = 0; t < 32; ++t) {
                ld16<0>(k1[0], vok, kb + t * 1024); ld8<0>(k2[0], vo8, k2b + t * 256);
                ldv(v0[0], v1[0], vov, vb + t * 1152);
                waitv<0>(k1[0], k2[0]); waitv<0>(v0[0], v1[0]);
#pragma unroll
                for (int qb = 0; qb < 4; ++qb) {
                    f32x16 c = MFMA32(k1[0], q1[qb], zero);
                    c = MFMA8(k2[0], q2[qb], c);
                    float mx = c[0];
#pragma unroll
                    for (int r = 1; r < 16; ++r) mx = fmaxf(mx, c[r]);
                    const float mn = fmaxf(mrow[qb], max32(mx)), al = __builtin_amdgcn_exp2f(mrow[qb] - mn);
                    mrow[qb] = mn;
                    u32x4 p0, p1;
#pragma unroll
                    for (int r = 0; r < 4; ++r) {
                        p0[r] = pack2(__builtin_amdgcn_exp2f(c[2 * r] - mn), __builtin_amdgcn_exp2f(c[2 * r + 1] - mn));
                        p1[r] = pack2(__builtin_amdgcn_exp2f(c[8 + 2 * r] - mn), __builtin_amdgcn_exp2f(c[9 + 2 * r] - mn));
                    }
#pragma unroll
                    for (int r = 0; r < 16; ++r) o[qb][r] *= al;
                    o[qb] = MFMA32(v0[0], __builtin_bit_cast(bf16x8, p0), o[qb]);
                    o[qb] = MFMA32(v1[0], __builtin_bit_cast(bf16x8, p1), o[qb]);
                }
            }
        }
        __builtin_amdgcn_s_setprio(MPRIO);
        const unsigned ex = lds0 + L_EX + vo16;
#pragma unroll
        for (int j = 0; j < 2; ++j) {
            const f32x16& s = kh ? o[2 * j] : o[2 * j + 1];
            const float mm = kh ? mrow[2 * j] : mrow[2 * j + 1];
            lds_st4(ex + a * 6144 + j * 3072, s[0], s[1], s[2], s[3]);
            lds_st4(ex + a * 6144 + j * 3072 + 1024, s[4], s[5], s[6], s[7]);
            lds_st4(ex + a * 6144 + j * 3072 + 2048, s[8], s[9], s[10], mm);
        }
        asm volatile("s_waitcnt lgkmcnt(0)" ::: "memory");
        cnt_post<C_X>(cb + 4 * par, one);
        cnt_wait<C_X, 0>(cb + 4 * par, 2);
#pragma unroll
        for (int j = 0; j < 2; ++j) {
            const f32x16& s = kh ? o[2 * j + 1] : o[2 * j];
            const float ma = kh ? mrow[2 * j + 1] : mrow[2 * j];
            const unsigned src = ex + (a ^ 2) * 6144 + j * 3072;
            const f32x4 t0 = lds_f4(src), t1 = lds_f4(src + 1024), t2 = lds_f4(src + 2048);
            const float mb = t2[3], mm = fmaxf(ma, mb);
            const float fa = __builtin_amdgcn_exp2f(ma - mm), fb = __builtin_amdgcn_exp2f(mb - mm);
            const float inv = __builtin_amdgcn_rcpf(low32(s[10] * fa + t2[2] * fb)), ia = fa * inv, ib = fb * inv;
            const u32x4 pk = {pack2(s[0] * ia + t0[0] * ib, s[1] * ia + t0[1] * ib), pack2(s[2] * ia + t0[2] * ib, s[3] * ia + t0[3] * ib),
                              pack2(s[4] * ia + t1[0] * ib, s[5] * ia + t1[1] * ib), pack2(s[6] * ia + t1[2] * ib, s[7] * ia + t1[3] * ib)};
            const int tb = 8 * mt + 4 * par + 2 * j + kh;
            st16(pk, rs, vo16, WS_O + (tb * 36 + nt) * 1024);
            __builtin_amdgcn_raw_buffer_store_b32(pack2(s[8] * ia + t2[0] * ib, s[9] * ia + t2[1] * ib), rs, hi ? 0x80000000u : vo16,
                                                  WS_O + (tb * 36 + 32 + sp) * 1024 + (nt & 4) * 128 + (nt & 1) * 8 + (nt & 2) * 2, SC1);
        }
        waitn<0>();
        flag_set(ws, WS_FO + ((mt * 32 + nt) * 4 + a) * FS, tag, lane);
        __builtin_amdgcn_s_setprio(0);
        const size_t orow = (size_t)((4 * mt2 + sp) * 32 + l31) * (DM * 2) + nt2 * 288 + hi * 16;
        u32x4 xr[3];
#pragma unroll
        for (int i = 0; i < 3; ++i) xr[i] = *(const u32x4*)(x + orow + (i < 2 ? a + 4 * i : 8) * 32);
        if (bid == 0 && a == 1) {
#pragma unroll
            for (int r = 0; r < 4; ++r) flag_wait(ws, WS_FX + (r * 64 + lane) * FS, tag);
            flag_set(ws, WS_EPOCH, tag - TAG_STEP + 1, lane);
        }
        cnt_wait<C_P, 4>(cb, 1);
        f32x4 s0[3], s1[3];
#pragma unroll
        for (int i = 0; i < 3; ++i) {
            const unsigned src = lds0 + G_Y + (i < 2 ? a + 4 * i : 8) * 2048 + lane * 32;
            s0[i] = lds_f4(src) + f32x4{blo(xr[i][0]), bhi(xr[i][0]), blo(xr[i][1]), bhi(xr[i][1])};
            s1[i] = lds_f4(src + 16) + f32x4{blo(xr[i][2]), bhi(xr[i][2]), blo(xr[i][3]), bhi(xr[i][3])};
            PIN(s0[i]); PIN(s1[i]);
        }
        flag_wait(ws, WS_FP + (bid * 4 + ((sp + 1 + min(lane, 2)) & 3)) * FPS, tag);
        u32x4 pp[3][3];
#pragma unroll
        for (int r = 0; r < 3; ++r)
#pragma unroll
            for (int i = 0; i < 3; ++i)
                pp[r][i] = __builtin_amdgcn_raw_buffer_load_b128(rs, (i < 2 || own8) ? vo16 : 0x80000000u,
                                                                 WS_P + (bid * 4 + ((sp + 1 + r) & 3)) * 9216 + (i < 2 ? a + 4 * i : 8) * 1024, SC1);
        SBAR();
#pragma unroll
        for (int i = 0; i < 3; ++i) {
#pragma unroll
            for (int r = 0; r < 3; ++r) {
                s0[i] += f32x4{blo(pp[r][i][0]), bhi(pp[r][i][0]), blo(pp[r][i][1]), bhi(pp[r][i][1])};
                s1[i] += f32x4{blo(pp[r][i][2]), bhi(pp[r][i][2]), blo(pp[r][i][3]), bhi(pp[r][i][3])};
            }
            if (i < 2 || own8)
                *(u32x4*)(out + orow + (i < 2 ? a + 4 * i : 8) * 32) = u32x4{pack2(s0[i][0], s0[i][1]), pack2(s0[i][2], s0[i][3]), pack2(s1[i][0], s1[i][1]), pack2(s1[i][2], s1[i][3])};
        }
    }
}

#ifdef __HIP_DEVICE_COMPILE__
#pragma clang attribute pop
#endif

extern "C" void kernel_launch(void* const* d_in, const int* in_sizes, int n_in, void* d_out, int out_size, void* d_ws, size_t ws_size,
                              hipStream_t stream) {
    (void)hipFuncSetAttribute((const void*)k_all, hipFuncAttributeMaxDynamicSharedMemorySize, LDS_BYTES);
    k_all<<<256, 512, LDS_BYTES, stream>>>((const char*)d_in[0], (const char*)d_in[1], (const char*)d_in[2], (const char*)d_in[3],
                                           (const char*)d_in[4], (const char*)d_in[6], (const char*)d_in[7], (const char*)d_in[8],
                                           (char*)d_ws, (char*)d_out);
}
```

```cpp
#include <hip/hip_runtime.h>
#include <stdint.h>

#ifdef __HIP_DEVICE_COMPILE__
#pragma clang attribute push(__attribute__((target("no-packed-fp32-ops"))), apply_to = function)
#endif

typedef __attribute__((ext_vector_type(8))) __bf16 bf16x8;
typedef __attribute__((ext_vector_type(2))) __bf16 bf16x2;
typedef __attribute__((ext_vector_type(4))) short s16x4;
typedef __attribute__((ext_vector_type(8))) short s16x8;
typedef __attribute__((ext_vector_type(4))) float f32x4;
typedef __attribute__((ext_vector_type(2))) float f32x2;
typedef __attribute__((ext_vector_type(16))) float f32x16;
typedef __attribute__((ext_vector_type(4))) unsigned u32x4;
typedef __attribute__((ext_vector_type(2))) unsigned u32x2;
typedef unsigned long long u64;
#define LDS_AS __attribute__((address_space(3)))
#define GLB_AS __attribute__((address_space(1)))

typedef __attribute__((ext_vector_type(3))) unsigned u32x3;

constexpr int DM = 576, FF = 4608;
constexpr unsigned FS = 64;
constexpr unsigned WS_EPOCH = 0, WS_FKV = 4096, WS_FX = 8192, WS_FO = WS_FX + 256 * FS, WS_FH = WS_FO + 1024 * FS, WS_FP = WS_FH + 2048 * FS;
constexpr int FPS = 128;
constexpr unsigned WS_XN = WS_FP + 1024 * 128;
constexpr unsigned WS_H = WS_XN + 64u * 36 * 1024;
constexpr unsigned WS_K1 = WS_H + 64u * 288 * 1024;
constexpr unsigned WS_K2 = WS_K1 + 64u * 1024;
constexpr unsigned WS_V = WS_K2 + 64u * 256;
constexpr unsigned WS_O = WS_V + 64u * 2048;
constexpr unsigned WS_P = WS_O + 64u * 36 * 1024;
constexpr unsigned WS_END = WS_P + 1024u * 9216;

constexpr int W_PART = 10368, W_SLOT = 2 * W_PART;
constexpr int L_KV = 3 * W_SLOT, L_WQ = L_KV + 2 * 27648;
constexpr int L_Q = L_KV + 4096, L_XS = 2 * W_SLOT;
constexpr int G_X = 64512, G_Y = G_X + 36864, G_Z = 0, G_L = 36864;
constexpr int L_EX = G_Y + 36864;
constexpr int L_CNT = L_EX + 24576;
constexpr int C_L = 0, C_C = 4, C_A = 8, C_S = 12, C_F = 16, C_B = 20, C_Q = 28, C_X = 36, C_P = 44, C_K = 24, C_TAG = 56;
constexpr int LDS_BYTES = 163840;
static_assert(L_WQ + 27648 <= L_CNT && L_Q >= G_X && 3 * W_SLOT <= G_X && L_CNT + 1024 <= LDS_BYTES && G_L + 9216 <= G_X, "lds map");

constexpr float SM_SAFE = 48.f;
constexpr int APRIO = 2;
constexpr unsigned SPIN_MAX = 1u << 21;
constexpr int GPRIO = 0;
constexpr int LOWT = 0;
constexpr int JSB = 12;
constexpr int KVW = 12;
constexpr int EPA = 2;
constexpr int CPF = 23;
constexpr int PFA = 0;
constexpr int PFB = 2;
constexpr int PFG = 9;
constexpr int W1E = 0;
constexpr int HNT = 0;
constexpr int P2 = 0;
constexpr int GD = 2;
constexpr int MPRIO = 3;
constexpr u64 TAG_STEP = 0x9e3779b97f4a7c15ull;

#define SBAR() __builtin_amdgcn_sched_barrier(0)
#define MFMA32(a, b, c) __builtin_amdgcn_mfma_f32_32x32x16_bf16(a, b, c, 0, 0, 0)
#define MFMA8(a, b, c) __builtin_amdgcn_mfma_f32_32x32x8bf16_1k(a, b, c, 0, 0, 0)
#define PIN(x) asm volatile("" : "+v"(x))
#define SC1 16

__device__ __forceinline__ float u2f(unsigned u) { return __builtin_bit_cast(float, u); }
__device__ __forceinline__ unsigned f2u(float f) { return __builtin_bit_cast(unsigned, f); }
__device__ __forceinline__ bool all_lanes(bool p) { return __builtin_amdgcn_ballot_w64(!p) == 0; }
template <int N> struct IC { static constexpr int v = N; };
template <int I, int N, class F> __device__ __forceinline__ void static_for(F&& f) {
    if constexpr (I < N) { f(IC<I>{}); static_for<I + 1, N>(f); }
}

__device__ __forceinline__ void set_m0(unsigned lds_base) { asm volatile("s_mov_b32 m0, %0\n\ts_nop 0" :: "s"(lds_base) : "memory"); }
template <int OFF> __device__ __forceinline__ void glds(unsigned voff, const char* sbase) {
    asm volatile("global_load_lds_dwordx4 %0, %1 offset:%2" :: "v"(voff), "s"(sbase), "n"(OFF) : "memory");
}
template <int OFF> __device__ __forceinline__ void ld16(bf16x8& d, unsigned voff, const char* sbase) {
    asm volatile("global_load_dwordx4 %0, %1, %2 offset:%3 sc1" : "=v"(d) : "v"(voff), "s"(sbase), "n"(OFF) : "memory");
}
template <int OFF> __device__ __forceinline__ void ld8(s16x4& d, unsigned voff, const char* sbase) {
    asm volatile("global_load_dwordx2 %0, %1, %2 offset:%3 sc1" : "=v"(d) : "v"(voff), "s"(sbase), "n"(OFF) : "memory");
}
__device__ __forceinline__ void ldv(bf16x8& a, bf16x8& b, unsigned voff, const char* sbase) {
    asm volatile("s_mov_b64 exec, %4\n\tglobal_load_dwordx4 %0, %2, %3 sc1\n\tglobal_load_dwordx4 %1, %2, %3 offset:576 sc1\n\ts_mov_b64 exec, -1"
                 : "+v"(a), "+v"(b) : "v"(voff), "s"(sbase), "s"(0x0003ffff0003ffffull) : "memory");
}
__device__ __forceinline__ void touch(unsigned& d, unsigned voff, const char* sbase) {
    asm volatile("global_load_dword %0, %1, %2" : "+v"(d) : "v"(voff), "s"(sbase) : "memory");
}
__device__ __forceinline__ void ldflag(u64& d, unsigned voff, const char* sbase) {
    asm volatile("global_load_dwordx2 %0, %1, %2 sc1" : "=v"(d) : "v"(voff), "s"(sbase) : "memory");
}
template <int N, class T> __device__ __forceinline__ void waitv(T& a) { asm volatile("s_waitcnt vmcnt(%1)" : "+v"(a) : "n"(N < 63 ? N : 63) : "memory"); }
template <int N, class T, class U> __device__ __forceinline__ void waitv(T& a, U& b) {
    asm volatile("s_waitcnt vmcnt(%2)" : "+v"(a), "+v"(b) : "n"(N < 63 ? N : 63) : "memory");
}
template <class T> __device__ __forceinline__ void pin(T& a) { asm volatile("" : "+v"(a) :: "memory"); }
template <int N> __device__ __forceinline__ void waitn() { asm volatile("s_waitcnt vmcnt(%0)" :: "n"(N < 63 ? N : 63) : "memory"); }

template <int AUX = SC1> __device__ __forceinline__ void st16(u32x4 v, __amdgpu_buffer_rsrc_t rs, unsigned voff, unsigned soff) {
    __builtin_amdgcn_raw_buffer_store_b128(v, rs, voff, soff, AUX);
    asm volatile("s_nop 1" :: "v"(v) : "memory");
}
__device__ __forceinline__ bf16x8 lds_tr2(unsigned a0, unsigned a1) {
    s16x4 lo = __builtin_amdgcn_ds_read_tr16_b64_v4i16((LDS_AS s16x4*)(size_t)a0);
    s16x4 hi = __builtin_amdgcn_ds_read_tr16_b64_v4i16((LDS_AS s16x4*)(size_t)a1);
    s16x8 r = __builtin_shufflevector(lo, hi, 0, 1, 2, 3, 4, 5, 6, 7);
    return __builtin_bit_cast(bf16x8, r);
}
__device__ __forceinline__ f32x4 lds_f4(unsigned a) { return *(const LDS_AS f32x4*)(size_t)a; }
__device__ __forceinline__ void lds_st4(unsigned a, float v0, float v1, float v2, float v3) { *(LDS_AS f32x4*)(size_t)a = f32x4{v0, v1, v2, v3}; }
__device__ __forceinline__ unsigned pack2(float lo, float hi) {
    bf16x2 v = {(__bf16)lo, (__bf16)hi};
    return __builtin_bit_cast(unsigned, v);
}
__device__ __forceinline__ float bf16r(float v) { return (float)(__bf16)v; }
__device__ __forceinline__ float blo(unsigned u) { return u2f(u << 16); }
__device__ __forceinline__ float bhi(unsigned u) { return u2f(u & 0xffff0000u); }
__device__ __forceinline__ float sum32(float a) {
    auto r = __builtin_amdgcn_permlane32_swap(f2u(a), f2u(a), false, false);
    return u2f(r[0]) + u2f(r[1]);
}
__device__ __forceinline__ float max32(float a) {
    auto r = __builtin_amdgcn_permlane32_swap(f2u(a), f2u(a), false, false);
    return fmaxf(u2f(r[0]), u2f(r[1]));
}
__device__ __forceinline__ float low32(float a) {
    auto r = __builtin_amdgcn_permlane32_swap(f2u(a), f2u(a), false, false);
    return u2f(r[0]);
}
__device__ __forceinline__ u64 uni64(u64 v) {
    return (u64)(unsigned)__builtin_amdgcn_readfirstlane((unsigned)(v >> 32)) << 32 | (unsigned)__builtin_amdgcn_readfirstlane((unsigned)v);
}
template <int CTRL> __device__ __forceinline__ float dpp(float a) { return u2f(__builtin_amdgcn_mov_dpp(f2u(a), CTRL, 0xf, 0xf, true)); }
__device__ __forceinline__ float half_sum(float a) {
    a += dpp<0xB1>(a); a += dpp<0x4E>(a); a += dpp<0x141>(a); a += dpp<0x140>(a);
    auto r = __builtin_amdgcn_permlane16_swap(f2u(a), f2u(a), false, false);
    return u2f(r[0]) + u2f(r[1]);
}
__device__ __forceinline__ float half_max(float a) {
    a = fmaxf(a, dpp<0xB1>(a)); a = fmaxf(a, dpp<0x4E>(a)); a = fmaxf(a, dpp<0x141>(a)); a = fmaxf(a, dpp<0x140>(a));
    auto r = __builtin_amdgcn_permlane16_swap(f2u(a), f2u(a), false, false);
    return fmaxf(u2f(r[0]), u2f(r[1]));
}
__device__ __forceinline__ unsigned cnt_add(unsigned addr) {
    unsigned old, r;
    asm volatile("s_mov_b64 exec, 1\n\tds_add_rtn_u32 %0, %2, %3\n\ts_waitcnt lgkmcnt(0)\n\tv_readfirstlane_b32 %1, %0\n\ts_mov_b64 exec, -1"
                 : "=&v"(old), "=s"(r) : "v"(addr), "v"(1u) : "memory");
    return r;
}
template <int OFF> __device__ __forceinline__ void cnt_post(unsigned base, unsigned one) {
    asm volatile("s_mov_b64 exec, 1\n\tds_add_u32 %0, %1 offset:%2\n\ts_mov_b64 exec, -1" :: "v"(base), "v"(one), "n"(OFF) : "memory");
}
template <int OFF, int SLEEP = 1> __device__ __forceinline__ void cnt_wait(unsigned base, unsigned target) {
    unsigned v, t, lim = SPIN_MAX * 8;
    asm volatile("1:\n\tds_read_b32 %0, %3 offset:%5\n\ts_waitcnt lgkmcnt(0)\n\tv_readfirstlane_b32 %1, %0\n\ts_cmp_ge_u32 %1, %4\n\ts_cbranch_scc1 2f\n\t"
                 "s_sleep %6\n\ts_sub_u32 %2, %2, 1\n\ts_cbranch_scc0 1b\n2:"
                 : "=&v"(v), "=&s"(t), "+s"(lim) : "v"(base), "s"(target), "n"(OFF), "n"(SLEEP) : "memory", "scc");
}
template <int OFF> __device__ __forceinline__ unsigned cnt_peek(unsigned base) { return *(volatile const LDS_AS unsigned*)(size_t)(base + OFF); }
template <int OFF, int TARGET> __device__ __forceinline__ void cnt_check(unsigned base, unsigned v) {
    unsigned t, lim;
    asm volatile("s_setprio 3\n\tv_readfirstlane_b32 %1, %0\n\ts_setprio %7\n\ts_cmp_ge_u32 %1, %4\n\ts_cbranch_scc1 2f\n\ts_mov_b32 %2, %6\n"
                 "1:\n\tds_read_b32 %0, %3 offset:%5\n\ts_waitcnt lgkmcnt(0)\n\tv_readfirstlane_b32 %1, %0\n\ts_cmp_ge_u32 %1, %4\n\ts_cbranch_scc1 2f\n\t"
                 "s_sub_u32 %2, %2, 1\n\ts_cbranch_scc0 1b\n2:"
                 : "+v"(v), "=&s"(t), "=&s"(lim) : "v"(base), "n"(TARGET), "n"(OFF), "n"(SPIN_MAX * 8), "n"(GPRIO) : "memory", "scc");
}
template <int OFF> __device__ __forceinline__ unsigned add_now(unsigned base) {
    unsigned r;
    asm volatile("s_setprio 3\n\tv_add_u32 %0, %2, %1\n\ts_setprio %3" : "=v"(r) : "v"(base), "n"(OFF), "n"(GPRIO));
    return r;
}
__device__ __forceinline__ void flag_set(char* ws, unsigned off, u64 tag, int lane) {
    if (lane == 0) __hip_atomic_store((GLB_AS u64*)(ws + off), tag, __ATOMIC_RELAXED, __HIP_MEMORY_SCOPE_AGENT);
}
__device__ __forceinline__ void flag_put(const char* ws, unsigned off, u64 tag) {
    unsigned o; u64 t;
    asm volatile("s_mov_b64 exec, 1\n\ts_setprio 3\n\tv_mov_b32 %0, %2\n\tv_mov_b64 %1, %3\n\ts_setprio %5\n\tglobal_store_dwordx2 %0, %1, %4 sc1\n\ts_mov_b64 exec, -1"
                 : "=&v"(o), "=&v"(t) : "s"(off), "s"(tag), "s"(ws), "n"(GPRIO) : "memory");
}
__device__ __forceinline__ void flag_wait(const char* ws, unsigned off, u64 tag) {
    u64 v; unsigned lim = SPIN_MAX;
    asm volatile("1:\n\tglobal_load_dwordx2 %0, %2, %3 sc1\n\ts_waitcnt vmcnt(0)\n\tv_cmp_ne_u64 vcc, %4, %0\n\ts_cmp_eq_u64 vcc, 0\n\ts_cbranch_scc1 2f\n\t"
                 "s_sleep 2\n\ts_sub_u32 %1, %1, 1\n\ts_cbranch_scc0 1b\n2:"
                 : "=&v"(v), "+s"(lim) : "v"(off), "s"(ws), "s"(tag) : "memory", "scc", "vcc");
}
template <int GAP> __device__ __forceinline__ void flag_wait2(const char* ws, unsigned off, u64 tag) {
    u64 v0, v1; unsigned lim = SPIN_MAX;
    asm volatile("global_load_dwordx2 %0, %3, %4 sc1\n\ts_sleep %6\n"
                 "1:\n\tglobal_load_dwordx2 %1, %3, %4 sc1\n\ts_waitcnt vmcnt(1)\n\tv_cmp_ne_u64 vcc, %5, %0\n\ts_cmp_eq_u64 vcc, 0\n\ts_cbranch_scc1 2f\n\t"
                 "global_load_dwordx2 %0, %3, %4 sc1\n\ts_waitcnt vmcnt(1)\n\tv_cmp_ne_u64 vcc, %5, %1\n\ts_cmp_eq_u64 vcc, 0\n\ts_cbranch_scc1 2f\n\t"
                 "s_sub_u32 %2, %2, 1\n\ts_cbranch_scc0 1b\n2:\n\ts_waitcnt vmcnt(0)"
                 : "=&v"(v0), "=&v"(v1), "+s"(lim) : "v"(off), "s"(ws), "s"(tag), "n"(GAP) : "memory", "scc", "vcc");
}
__device__ __forceinline__ void flag_check(const char* ws, unsigned off, u64 tag, u64 v) {
    unsigned lim;
    asm volatile("s_setprio 3\n\tv_cmp_ne_u64 vcc, %4, %0\n\ts_setprio %6\n\ts_cmp_eq_u64 vcc, 0\n\ts_cbranch_scc1 2f\n\ts_mov_b32 %1, %5\n"
                 "1:\n\tglobal_load_dwordx2 %0, %2, %3 sc1\n\ts_waitcnt vmcnt(0)\n\tv_cmp_ne_u64 vcc, %4, %0\n\ts_cmp_eq_u64 vcc, 0\n\ts_cbranch_scc1 2f\n\t"
                 "s_sleep 1\n\ts_sub_u32 %1, %1, 1\n\ts_cbranch_scc0 1b\n2:"
                 : "+v"(v), "=&s"(lim) : "v"(off), "s"(ws), "s"(tag), "n"(SPIN_MAX), "n"(GPRIO) : "memory", "scc", "vcc");
}
__device__ __forceinline__ void swiglu4(unsigned& o0, unsigned& o1, float g0, float g1, float g2, float g3, float u0, float u1, float u2, float u3) {
    float t0, t1, t2, t3, p0, p1, p2, p3;
    asm("v_mul_f32 %2, 0xbfb8aa3b, %10\n\tv_mul_f32 %3, 0xbfb8aa3b, %11\n\tv_mul_f32 %4, 0xbfb8aa3b, %12\n\tv_mul_f32 %5, 0xbfb8aa3b, %13\n\t"
        "v_exp_f32 %2, %2\n\tv_exp_f32 %3, %3\n\tv_exp_f32 %4, %4\n\tv_exp_f32 %5, %5\n\t"
        "v_mul_f32 %6, %10, %14\n\tv_mul_f32 %7, %11, %15\n\tv_mul_f32 %8, %12, %16\n\tv_mul_f32 %9, %13, %17\n\t"
        "v_add_f32 %2, 1.0, %2\n\tv_add_f32 %3, 1.0, %3\n\tv_add_f32 %4, 1.0, %4\n\tv_add_f32 %5, 1.0, %5\n\t"
        "v_rcp_f32 %2, %2\n\tv_rcp_f32 %3, %3\n\tv_rcp_f32 %4, %4\n\tv_rcp_f32 %5, %5\n\t"
        "v_mul_f32 %6, %6, %2\n\tv_mul_f32 %7, %7, %3\n\tv_mul_f32 %8, %8, %4\n\tv_mul_f32 %9, %9, %5\n\t"
        "v_cvt_pk_bf16_f32 %0, %6, %7\n\tv_cvt_pk_bf16_f32 %1, %8, %9"
        : "=v"(o0), "=v"(o1), "=&v"(t0), "=&v"(t1), "=&v"(t2), "=&v"(t3), "=&v"(p0), "=&v"(p1), "=&v"(p2), "=&v"(p3)
        : "v"(g0), "v"(g1), "v"(g2), "v"(g3), "v"(u0), "v"(u1), "v"(u2), "v"(u3));
}

__device__ __forceinline__ int kperm(int r) { return (r & 8) + ((r & 1) << 2) + ((r >> 1) & 3); }

constexpr int S_FA = 42, S_SA = 60, S_CA = 66;
constexpr int g1_piece(int s, int u) { const int U = (s & 3) * 5 + u; return (U >= 2 && U <= ((s >> 2) <= 15 ? 6 : 10)) || (PFG && U == 13 && (s >> 2) >= PFG) ? 1 : 0; }
constexpr int g1_cum(int s, int u) {
    int n = 0;
    for (int t = 0; t <= s; ++t) {
        const int e = t < s ? 5 : u;
        if (e > 0 && t == S_FA) ++n;
        for (int i = 0; i < e; ++i) n += g1_piece(t, i);
        if (t < s) n += 2 + (t == S_SA ? 1 : 0) + (t == 35 ? 8 : 0);
    }
    return n;
}
constexpr int g1_upto_loads(int s) { return g1_cum(s + 1, 0) - (s == 35 ? 8 : 0); }
constexpr int g1_upto_unit(int T, int U) { return g1_cum(4 * T + U / 5, U % 5 + 1); }
constexpr int J_FB = 3, J_SB = JSB, J_CB = 16, J_SO = 58, J_CO = 63, HR = 16;
constexpr int G2_BASE = g1_cum(72, 0) + 10 + (HR - 12);
constexpr int g2_pieces(int J) { const int j = J & 7; return (J < 64 ? (j >= 1 && j <= 4 ? 2 : j == 5 ? 1 : 0) : 0) + (J == 6 ? 2 : 0); }
constexpr int g2_next(int J) { return J < 72 - HR ? J + HR : (J >= 64 && J <= 72) ? J + 8 : -1; }
constexpr int g2_misc(int J) { return (J == J_FB || J == J_SB || J == J_SO) ? 1 : 0; }
constexpr int g2_cum(int J) { int n = G2_BASE; for (int t = 0; t < J; ++t) n += g2_pieces(t) + g2_misc(t) + (g2_next(t) >= 0 ? 1 : 0); return n; }
constexpr int g2_upto_frag(int J) {
    if (J < 12) return g1_upto_loads(66 + J / 2) - ((J & 1) ? 0 : 1);
    if (J < HR) return G2_BASE - (HR - 1 - J);
    return g2_cum((J < 72 ? J - HR : J - 8) + 1);
}
constexpr int g2_slot(int T) { return T % 3 == 0 ? G_X : T % 3 == 1 ? G_Y : G_Z; }

__global__ __launch_bounds__(512, 2) void k_all(const char* __restrict__ x, const char* __restrict__ wq, const char* __restrict__ wk,
                                                const char* __restrict__ wv, const char* __restrict__ wo, const char* __restrict__ w1,
                                                const char* __restrict__ w3, const char* __restrict__ w2, char* ws, char* __restrict__ out) {
    extern __shared__ __attribute__((aligned(16))) char smem[];
    u64 pc;
    asm volatile("s_getpc_b64 %0" : "=s"(pc));
    const unsigned lds0 = (unsigned)(size_t)(LDS_AS char*)smem;
    const int lane = threadIdx.x & 63, w = __builtin_amdgcn_readfirstlane(threadIdx.x >> 6);
    const int bid = blockIdx.x;
    const int l31 = lane & 31, hi = lane >> 5;
    const __amdgpu_buffer_rsrc_t rs = __builtin_amdgcn_make_buffer_rsrc(ws, 0, WS_END, 0x00020000);
    const int th = bid & 1, sp = (bid >> 1) & 3, r_ = bid >> 3;
    const int mt = 4 * th + (r_ >> 3), nt = 8 * sp + (r_ & 7);
    const int mt2 = 8 * th + (r_ >> 2), nt2 = r_ & 3;
    unsigned pc0;
    if (CPF) {
        const unsigned vo = (r_ * CPF + min(lane, CPF - 1)) * 128;
        asm volatile("s_cmp_lg_u32 %3, 0\n\ts_cbranch_scc1 1f\n\tglobal_load_dword %0, %1, %2\n1:" : "=v"(pc0) : "v"(vo), "s"(pc), "s"(w) : "memory", "scc");
    }
    unsigned cb = lds0 + L_CNT, one = 1u, vo16 = lane * 16;
    PIN(cb); PIN(one); PIN(vo16);
    auto q_groups = [&](int g0, int n) {
        unsigned vi[3];
#pragma unroll
        for (int p = 0; p < 3; ++p) { const unsigned idx = 64 * p + lane, row = idx / 3, c = idx - row * 3; vi[p] = row * (DM * 2) + (c == 2 ? 20 : 16 * c) - p * 1024; }
#pragma unroll
        for (int g = 0; g < n; ++g) {
            const char* src = wq + nt * 36 + (g0 + g) * (64 * DM * 2);
            set_m0(lds0 + L_WQ + (g0 + g) * 3072);
            glds<0>(vi[0], src); glds<1024>(vi[1], src); glds<2048>(vi[2], src);
        }
    };
    u32x3 r[3];
    u64 tag;
    asm volatile("s_cmp_lt_u32 %8, 4\n\ts_cbranch_scc1 1f\n\t"
                 "global_load_dwordx3 %0, %4, %5\n\tglobal_load_dwordx3 %1, %4, %5 offset:384\n\tglobal_load_dwordx3 %2, %4, %5 offset:768\n\t"
                 "global_load_dwordx2 %3, %6, %7 sc1\n\ts_branch 2f\n"
                 "1:\n\ts_cmp_lg_u32 %8, 0\n\ts_cbranch_scc1 2f\n\ts_mov_b64 exec, 0x3fff\n\tds_write_b32 %9, %6\n\ts_mov_b64 exec, -1\n\ts_waitcnt lgkmcnt(0)\n"
                 "2:\n\ts_barrier"
                 : "=&v"(r[0]), "=&v"(r[1]), "=&v"(r[2]), "=&v"(tag)
                 : "v"((2 * (w & 3) + hi) * (DM * 2) + l31 * 12), "s"(x + (size_t)bid * 8 * DM * 2), "v"(WS_EPOCH), "s"(ws), "s"(w), "v"(cb + 4 * lane) : "memory", "scc");
    if (w < 4) {
        const char* wsrc = ((w >> 1) ? w3 : w1) + (size_t)(32 * (w & 1)) * (FF * 2);
        const char* wsrcA = wsrc + 128 * nt;
        const char* wsrcB = wsrc + 4096 + 160 * nt;
        const unsigned dstW = lds0 + (w >> 1) * W_PART + (w & 1) * 5120;
        unsigned voffW[9];
#pragma unroll
        for (int p = 0; p < 5; ++p) { const unsigned idx = 64 * p + lane, row = idx / 10; voffW[p] = row * (FF * 2) + ((idx - row * 10) & 7) * 16 - (p & 3) * 1024; }
        auto w_piece = [&](auto TT, auto PP) {
            constexpr int T = TT.v, p = PP.v;
            if constexpr ((p & 3) == 0) set_m0(dstW + (T % 3) * W_SLOT + p * 1024);
            glds<(p & 3) * 1024>(voffW[p], T < 9 ? wsrcA + (size_t)T * 64 * FF * 2 : wsrcB + (size_t)(T - 9) * 64 * FF * 2);
        };
        unsigned pf;
        {
            const unsigned idx = min(16 * w + lane, 63) % 54, row = idx / 3;
            const unsigned vo = (18 * r_ + row) * (DM * 2) + ((sp * 288) & ~127) + (idx - row * 3) * 128;
            asm volatile("s_mov_b64 exec, %3\n\tglobal_load_dword %0, %1, %2\n\ts_mov_b64 exec, -1" : "=v"(pf) : "v"(vo), "s"(wq), "s"(0xffffull) : "memory");
        }
        if (GD == 1) cnt_wait<C_C, 2>(cb, 2);
        if (GD) { static_for<0, 5>([&](auto P) { w_piece(IC<0>{}, P); }); q_groups(w, 1); }
        if (GD == 2) cnt_wait<C_C, 2>(cb, 2);
        {
            const char* isrc = ((w >> 1) ? wv : wk) - 2048 + (w & 1) * (256 * 36);
            unsigned vi[3];
#pragma unroll
            for (int p = 0; p < 3; ++p) { const unsigned idx = 64 * p + lane, row = idx / 3, c = idx - row * 3; vi[p] = row * 36 + (c == 2 ? 20 : 16 * c) + 2048 - p * 1024; }
#pragma unroll
            for (int g = 0; g < 5; ++g) {
                set_m0(lds0 + L_KV + (w >> 1) * 27648 + ((w & 1) * 4 + g) * 3072);
                glds<0>(vi[0], isrc + g * 2304); glds<1024>(vi[1], isrc + g * 2304); glds<2048>(vi[2], isrc + g * 2304);
            }
        }
        if (!GD) static_for<0, 5>([&](auto P) { w_piece(IC<0>{}, P); });
        if (W1E == 1) static_for<0, 5>([&](auto P) { w_piece(IC<1>{}, P); });
        const unsigned wb = lds0 + ((lane >> 4) & 1) * W_PART + (8 * hi + ((lane >> 2) & 3)) * 160 + (lane & 3) * 8;
        f32x16 acc[5][2];
        if (!GD) q_groups(w, 1);
        const unsigned wqb = lds0 + L_WQ + (8 * hi + ((lane >> 2) & 3)) * 48 + ((lane & 16) ? 40 : 8 * (lane & 3));
        waitn<(W1E == 1 ? 5 : 0) + (GD ? 0 : 8)>();
        cnt_post<C_K>(cb, one);
        if (W1E == 2) static_for<0, 5>([&](auto P) { w_piece(IC<1>{}, P); });
        if (PFA == 2) {
            const unsigned l = min(lane, 15);
            touch(pf, l < 7 ? (128 + 14 * r_ + 7 * (w & 1) + l) * (FF * 2) + 1024 * sp : (18 * r_ + 9 * (w & 1) + l - 7) * (FF * 2) + 4096 + 1280 * sp, (w >> 1) ? w3 : w1);
        }
        if (PFA == 3) {
            const unsigned idx = min((w & 1) * 64 + lane, 111);
            touch(pf, (128 + 14 * r_ + (idx >> 3)) * (FF * 2) + 1024 * sp + (idx & 7) * 128, (w >> 1) ? w3 : w1);
        }
        if (PFA == 1) {
            const unsigned idx = min(lane, 15);
            touch(pf, (128 + 4 * r_ + 2 * (w & 1) + (idx >> 3)) * (FF * 2) + 1024 * sp + (idx & 7) * 128, (w >> 1) ? w3 : w1);
        }
        cnt_wait<C_S>(cb, 4);
        tag = uni64(*(volatile const LDS_AS u64*)(size_t)(cb + C_TAG));
        if (P2 & 1) flag_wait2<12>(ws, WS_FX + (mt * 32 + 8 * w + (lane & 7)) * FS, tag);
        else flag_wait(ws, WS_FX + (mt * 32 + 8 * w + (lane & 7)) * FS, tag);
        PIN(pf);
        if (CPF) PIN(pc0);
        cnt_post<C_L>(cb, one);
        const int tb0 = mt * 8 + 2 * w;
        const char* xb = ws + WS_XN + (size_t)tb0 * 36864;
        unsigned vo16b = vo16 + 36864;
        PIN(vo16b);
        bf16x8 xr[6][2], wf[5], hr[HR];
        auto x_load = [&](auto SS) {
            constexpr int ks = SS.v % 36;
            ld16<(ks & 3) * 1024>(xr[SS.v % 6][0], vo16, xb + (ks >> 2) * 4096);
            ld16<(ks & 3) * 1024>(xr[SS.v % 6][1], vo16b, xb + (ks >> 2) * 4096);
        };
        static_for<0, 6>([&](auto S) { x_load(S); });
        if (W1E == 0) static_for<0, 5>([&](auto P) { w_piece(IC<1>{}, P); });
        const int tbg = 4 * mt2 + w;
        const char* hbA = ws + WS_H + (size_t)(tbg * 288 + 32 * sp) * 1024;
        const char* hbB = ws + WS_H + (size_t)(tbg * 288 + 128 + 40 * sp) * 1024;
        const char* ob = ws + WS_O + (size_t)(tbg * 36) * 1024;
        auto h_load = [&](auto JJ) {
            constexpr int J = JJ.v, r = J < 32 ? J : J < 72 ? J - 32 : J - 72;
            if constexpr (J == 80) ld16<0>(hr[J % HR], vo16, ob + (32 + sp) * 1024);
            else ld16<(r & 3) * 1024>(hr[J % HR], vo16, (J < 32 ? hbA : J < 72 ? hbB : ob + sp * 8192) + (r >> 2) * 4096);
        };
        const char* w2A = w2 + ((size_t)(512 * sp + 32 * w) * DM + nt2 * 144) * 2;
        const char* w2B = w2 + ((size_t)(2048 + 640 * sp + 32 * w) * DM + nt2 * 144) * 2;
        const char* woS = wo + ((size_t)(18 * (8 * sp + 2 * w)) * DM + nt2 * 144) * 2;
        const unsigned dst2 = lds0 + w * 9216;
        auto w2_piece = [&](auto TT, auto PP) {
            constexpr int T = TT.v, p = PP.v, off = (p & 3) * 1024;
            if constexpr (off == 0) set_m0(dst2 + g2_slot(T) + p * 1024);
            if constexpr (T < 9) glds<off>(voffW[p], T < 4 ? w2A + (size_t)T * 128 * DM * 2 : w2B + (size_t)(T - 4) * 128 * DM * 2);
            else if constexpr (p == 4) {
                __builtin_amdgcn_s_setprio(3);
                SBAR();
                unsigned vo = voffW[p] + (vo16 >> 9) * (2 * DM * 2);
                PIN(vo);
                SBAR();
                __builtin_amdgcn_s_setprio(GPRIO);
                glds<off>(vo, woS);
            }
            else glds<off>(voffW[p], woS + (p > 4 ? 2 * DM * 2 : 0));
        };
        const unsigned fh = WS_FH + ((tbg >> 1) * 32 + 8 * sp + (lane & 7)) * FS;
        u64 fs;
        unsigned pk_c = 0, pk_l = 0;
        cnt_wait<C_L>(cb, 4);
        __builtin_amdgcn_s_setprio(GPRIO);
        const f32x16 zero = {};
        auto epilogue = [&](auto NBB, int ks0) {
            __builtin_amdgcn_s_setprio(NBB.v == 4 ? EPA : 3);
#pragma unroll
            for (int m = 0; m < 2; ++m)
#pragma unroll
                for (int u = 0; u < NBB.v; ++u) {
                    const f32x16& c = acc[u + 5 - NBB.v][m];
                    unsigned a0, a1, a2, a3;
                    swiglu4(a0, a1, c[0], c[1], c[2], c[3], c[8], c[9], c[10], c[11]);
                    swiglu4(a2, a3, c[4], c[5], c[6], c[7], c[12], c[13], c[14], c[15]);
                    st16<SC1 | (HNT & 1) * 2>(u32x4{a0, a1, a2, a3}, rs, vo16, WS_H + ((tb0 + m) * 288 + ks0 + u) * 1024);
                    SBAR();
                }
            __builtin_amdgcn_s_setprio(GPRIO);
        };
        static_for<0, 72>([&](auto SS) {
            constexpr int s = SS.v, NB = 5, NBUF = 5, T = s >> 2, sl = s % 36, UPS = 4 * NB;
            auto w_read = [&](auto GG) {
                constexpr int G = GG.v, st = (s / 36) * 36 + G / NB, off = ((st >> 2) % 3) * W_SLOT + (st & 3) * 2560 + (G % NB - (s < 36)) * 32;
                if constexpr (s < 36 && G % NB == 0) wf[G % NBUF] = lds_tr2(wqb + st * 768, wqb + st * 768 + 192);
                else wf[G % NBUF] = lds_tr2(wb + off, wb + off + 640);
            };
            if constexpr (sl == 0) { static_for<0, 4>([&](auto G) { w_read(G); }); SBAR(); }
            static_for<0, NB>([&](auto UU) {
                constexpr int u = UU.v, U = (s & 3) * NB + u, G = sl * NB + u;
                if constexpr (u == 0 && s < 6) waitv<g1_cum(s, 0) + 2 * (5 - s) + (W1E ? 0 : 5)>(xr[s][0], xr[s][1]);
                if constexpr (u == 0 && s >= 6) {
                    if constexpr (s == S_CA) {
                        asm volatile("s_waitcnt vmcnt(%3)" : "+v"(xr[s % 6][0]), "+v"(xr[s % 6][1]), "+v"(fs) : "n"(g1_cum(s, 0) - g1_upto_loads(s - 6)) : "memory");
                        flag_check(ws, fh, tag, fs);
                    } else waitv<g1_cum(s, 0) - g1_upto_loads(s - 6)>(xr[s % 6][0], xr[s % 6][1]);
                    if constexpr (s == S_FA) flag_put(ws, WS_FH + ((mt * 4 + w) * 32 + nt) * FS, tag);
                }
                if constexpr (T < 17 && U == 12) {
                    waitn<g1_cum(s, u) - (T >= 1 ? g1_upto_unit(T - 1, 6) : 0)>();
                    cnt_post<C_L>(cb, one);
                }
                if constexpr (T == 17 && U == 15) { waitn<g1_cum(s, u) - g1_upto_unit(16, 10)>(); cnt_post<C_L>(cb, one); }
                if constexpr (U == 2) cnt_check<C_C, 4 * (T + 1)>(cb, pk_c);
                if constexpr (T == 3 && U == 3) cnt_wait<C_F>(cb, 2);
                if constexpr (T == 16 && U == 2) cnt_wait<C_A>(cb, 4);
                if constexpr (T < 17 && T != 8 && U == UPS - 4) cnt_check<C_L, 4 * (T + 2)>(cb, pk_l);
                if constexpr (U == 1) pk_c = cnt_peek<C_C>(cb);
                if constexpr (T < 17 && U == (T == 8 ? UPS - 1 : UPS - 5)) pk_l = cnt_peek<C_L>(cb);
                if constexpr (G + 4 < 36 * NB) w_read(IC<G + 4>{});
                if constexpr (T == 7 && U == 1) {
                    __builtin_amdgcn_s_setprio(3);
                    unsigned ln = vo16; PIN(ln); ln >>= 4;
#pragma unroll
                    for (int p = 0; p < 5; ++p) { const unsigned idx = 64 * p + ln; voffW[p] += (idx % 10 >= 8) ? 128 : 0; PIN(voffW[p]); }
                    __builtin_amdgcn_s_setprio(GPRIO);
                }
                if constexpr (T == 16 && U == 1) {
                    __builtin_amdgcn_s_setprio(3);
                    unsigned ln = vo16; PIN(ln); ln >>= 4;
#pragma unroll
                    for (int p = 0; p < 9; ++p) {
                        const unsigned idx = 64 * p + ln, r = idx / 18;
                        voffW[p] = ((r & 16) + kperm(r & 15)) * (DM * 2) + (idx - r * 18) * 16 - (p & 3) * 1024;
                        PIN(voffW[p]);
                    }
                    __builtin_amdgcn_s_setprio(GPRIO);
                }
                SBAR();
                acc[u][0] = MFMA32(wf[G % NBUF], xr[s % 6][0], sl == 0 ? zero : acc[u][0]);
                SBAR();
                if constexpr (U == UPS - 1) cnt_post<C_C>(cb, one);
                if constexpr (g1_piece(s, u)) {
                    if constexpr (U == 13) {
                        const unsigned b = min((T - PFG) * 128 + 4 * r_ + w, 1295) * 1024;
                        set_m0(lds0 + L_EX);
                        asm volatile("global_load_lds_dword %0, %1" :: "v"(vo16), "s"(w2 + b + (b < 512 * DM * 2 ? 512 * sp : 1536 + 640 * sp) * (DM * 2)) : "memory");
                    } else if constexpr (T <= 15) w_piece(IC<T + 2>{}, IC<U - 2>{});
                    else w2_piece(IC<T - 16>{}, IC<U - 2>{});
                }
                SBAR();
                acc[u][1] = MFMA32(wf[G % NBUF], xr[s % 6][1], sl == 0 ? zero : acc[u][1]);
                SBAR();
                if constexpr (s == 35 && u == 1) {
                    constexpr float QS = 0.23570226039551584f * 1.4426950408889634f;
                    __builtin_amdgcn_s_setprio(3);
#pragma unroll
                    for (int m = 0; m < 2; ++m) {
                        const f32x16& c = acc[0][m];
                        const unsigned dq = lds0 + L_Q + (2 * w + m) * 1280;
                        *(LDS_AS u32x4*)(size_t)(dq + vo16) = u32x4{pack2(c[0] * QS, c[1] * QS), pack2(c[2] * QS, c[3] * QS), pack2(c[4] * QS, c[5] * QS), pack2(c[6] * QS, c[7] * QS)};
                        *(LDS_AS unsigned*)(size_t)(dq + 1024 + (vo16 >> 2)) = pack2(c[10] * QS, c[11] * QS);
                    }
                    asm volatile("s_waitcnt lgkmcnt(0)" ::: "memory");
                    cnt_post<C_Q>(cb + 4 * (w >> 1), one);
                    __builtin_amdgcn_s_setprio(GPRIO);
                    SBAR();
                }
            });
            if constexpr (s == S_SA) ldflag(fs, fh, ws);
            if constexpr (s < 66) x_load(IC<s + 6>{});
            else { h_load(IC<2 * (s - 66)>{}); h_load(IC<2 * (s - 66) + 1>{}); }
            SBAR();
            if constexpr (s == 35) {
                epilogue(IC<4>{}, 4 * nt);
                cnt_check<C_L, 40>(cb, pk_l);
            }
        });
        __builtin_amdgcn_s_setprio(3);
        unsigned ln = vo16; PIN(ln); ln >>= 4;
        unsigned trl = lds0 + (2 * ((ln >> 2) & 3) + (ln >> 5)) * 288 + ((ln >> 4) & 1) * 32 + (ln & 3) * 8, ra = trl + G_X;
        unsigned fo = WS_FO + (((tbg >> 3) * 32 + 8 * sp + (ln & 7)) * 4 + ((tbg >> 2) & 1) + 2 * (tbg & 1)) * FS;
        PIN(trl); PIN(ra); PIN(fo);
        cnt_wait<C_L>(cb, 76);
        epilogue(IC<5>{}, 128 + 5 * nt);
        static_for<12, HR>([&](auto J) { h_load(J); });
        f32x16 ac[5];
        bf16x8 vf[2][5];
#pragma unroll
        for (int n = 0; n < 5; ++n) vf[0][n] = lds_tr2(ra + n * 64, ra + n * 64 + 2304);
        static_for<0, 81>([&](auto JJ) {
            constexpr int J = JJ.v, T = J >> 3, j = J & 7;
            if constexpr (j == 5 && T < 9) {
                waitn<g2_cum(J) - (T == 0 ? g1_upto_unit(17, 10) : g2_cum(J - 8) + g2_pieces(J - 8))>();
                cnt_post<C_L>(cb, one);
            }
            if constexpr (j == 1 && T < 9) cnt_check<C_C, 4 * (19 + T)>(cb, pk_c);
            if constexpr (j == 7 && T < 9) {
                asm volatile("s_waitcnt lgkmcnt(0)" ::: "memory");
                if constexpr (T < 8) cnt_post<C_C>(cb, one);
                cnt_check<C_L, 4 * (20 + T)>(cb, pk_l);
                ra = add_now<g2_slot(T + 1)>(trl);
            }
            if constexpr (J == 79) ra = add_now<G_L>(trl);
            waitv<g2_cum(J) - g2_upto_frag(J)>(hr[J % HR]);
            if constexpr (j == 0 && T < 9) pk_c = cnt_peek<C_C>(cb);
            if constexpr (j == 6 && T < 9) pk_l = cnt_peek<C_L>(cb);
            SBAR();
            static_for<0, 5>([&](auto NN) {
                constexpr int n = NN.v, nx = ((J + 1) & 7) * 4608 + n * 64;
                if constexpr (J < 80) vf[(J + 1) & 1][n] = lds_tr2(ra + nx, ra + nx + 2304);
                SBAR();
                ac[n] = MFMA32(vf[J & 1][n], hr[J % HR], J == 0 ? zero : ac[n]);
                SBAR();
                if constexpr (J < 64 && j >= 1 && j <= 5 && (n == 1 || (n == 3 && j < 5))) w2_piece(IC<T + 2>{}, IC<2 * (j - 1) + n / 2>{});
                if constexpr (J == 6 && (n == 1 || n == 3)) {
                    __builtin_amdgcn_s_setprio(3);
                    SBAR();
                    unsigned ln = vo16; PIN(ln); ln >>= 4;
                    const unsigned pc = n == 1 ? w : 4, idx = min(64 * pc + ln, 287u), r = idx / 18, a_ = r >> 3, b_ = r & 1, c_ = (r >> 1) & 3;
                    unsigned vo = (18 * (4 * b_ + a_ + (c_ & 2)) + 16 + (c_ & 1)) * (DM * 2) + (idx - r * 18) * 16;
                    PIN(vo);
                    SBAR();
                    __builtin_amdgcn_s_setprio(GPRIO);
                    set_m0(lds0 + G_L + pc * 1024);
                    glds<0>(vo, wo + ((size_t)(144 * sp) * DM + nt2 * 144) * 2);
                }
                SBAR();
            });
            if constexpr (J == J_FB) {
                waitn<g2_cum(J) + g2_pieces(J) - (g1_cum(72, 0) + 10)>();
                flag_put(ws, WS_FH + ((32 + mt * 4 + w) * 32 + nt) * FS, tag);
            }
            if constexpr (J == J_SB) ldflag(fs, fh, ws + 1024 * FS);
            if constexpr (J == J_CB) { waitv<g2_cum(J) + g2_pieces(J) - (g2_cum(J_SB) + g2_pieces(J_SB) + 1)>(fs); flag_check(ws + 1024 * FS, fh, tag, fs); }
            if constexpr (J == J_SO) ldflag(fs, fo, ws);
            if constexpr (J == J_CO) {
                waitv<g2_cum(J) + g2_pieces(J) - (g2_cum(J_SO) + g2_pieces(J_SO) + 1)>(fs);
                flag_check(ws, fo, tag, fs);
            }
            if constexpr (g2_next(J) >= 0) h_load(IC<g2_next(J)>{});
            SBAR();
        });
        __builtin_amdgcn_s_setprio(3);
        if (w != sp) {
            const unsigned dst = ((bid & ~6) | (w << 1)) * 4 + sp;
#pragma unroll
            for (int g = 0; g < 9; ++g) {
                const f32x16& a = ac[g >> 1];
                const int o = 8 * (g & 1);
                auto r0 = __builtin_amdgcn_permlane32_swap(pack2(a[o], a[o + 1]), pack2(a[o + 4], a[o + 5]), false, false);
                auto r1 = __builtin_amdgcn_permlane32_swap(pack2(a[o + 2], a[o + 3]), pack2(a[o + 6], a[o + 7]), false, false);
                st16<SC1 | (HNT & 2)>(u32x4{r0[0], r1[0], r0[1], r1[1]}, rs, vo16, WS_P + dst * 9216 + g * 1024);
            }
            waitn<0>();
            flag_set(ws, WS_FP + dst * FPS, tag, vo16);
        } else {
#pragma unroll
            for (int c = 0; c < 18; ++c) {
                const f32x16& a = ac[c >> 2];
                lds_st4(lds0 + G_Y + c * 1024 + (vo16 & 496) * 2 + (vo16 >> 9) * 16, a[4 * (c & 3)], a[4 * (c & 3) + 1], a[4 * (c & 3) + 2], a[4 * (c & 3) + 3]);
            }
            asm volatile("s_waitcnt lgkmcnt(0)" ::: "memory");
            cnt_post<C_P>(cb, one);
        }
    } else {
        const int a = w - 4, par = a & 1, kh = a >> 1, kh2 = a & 1;
        const bool own8 = (l31 >> 3) == a;
        __builtin_amdgcn_s_setprio(3);
        {
            asm volatile("s_waitcnt vmcnt(0)" : "+v"(r[0]), "+v"(r[1]), "+v"(r[2]), "+v"(tag) :: "memory");
            float v[18], s = 0.f, q = 0.f;
#pragma unroll
            for (int i = 0; i < 9; ++i) { v[2 * i] = blo(r[i / 3][i % 3]); v[2 * i + 1] = bhi(r[i / 3][i % 3]); s += v[2 * i] + v[2 * i + 1]; }
            const float mu = half_sum(s) * (1.f / DM), mub = bf16r(mu);
#pragma unroll
            for (int i = 0; i < 18; ++i) q = fmaf(v[i] - mu, v[i] - mu, q);
            const float rstd = __builtin_amdgcn_rcpf(bf16r(__builtin_amdgcn_sqrtf(bf16r(bf16r(half_sum(q) * (1.f / DM)) + 1e-5f))));
            const unsigned dst = lds0 + L_XS + (2 * a + hi) * 1168 + l31 * 12;
#pragma unroll
            for (int i = 0; i < 9; ++i) {
                const unsigned d = pack2(v[2 * i] - mub, v[2 * i + 1] - mub);
                *(LDS_AS unsigned*)(size_t)(dst + (i / 3) * 384 + (i % 3) * 4) = pack2(blo(d) * rstd, bhi(d) * rstd);
            }
            tag = uni64(tag) + TAG_STEP;
            if (a == 0 && lane == 0) *(LDS_AS u64*)(size_t)(cb + C_TAG) = tag;
            asm volatile("s_waitcnt lgkmcnt(0)" ::: "memory");
            cnt_post<C_S>(cb, one);
            cnt_wait<C_S, 0>(cb, 4);
            if (__builtin_expect(a >= 2, 1)) {
#pragma unroll
                for (int i = 0; i < 5; ++i) {
                    const u32x4 d = *(const LDS_AS u32x4*)(size_t)(lds0 + L_XS + (lane & 7) * 1168 + ((4 * kh2 + i) * 8 + (lane >> 3)) * 16);
                    st16(d, rs, (lane >> 3) * 512 + (lane & 7) * 16, WS_XN + (bid >> 2) * 36864 + (bid & 3) * 128 + (4 * kh2 + i) * 4096);
                }
                cnt_post<C_C>(cb, one);
                waitn<0>();
                if (cnt_add(cb + C_B) == 1) flag_set(ws, WS_FX + bid * FS, tag, lane);
                q_groups(4 + 2 * kh2, 3);
                waitn<0>();
                cnt_post<C_F>(cb, one);
            } else {
                const unsigned ba = lds0 + L_KV + a * 27648 + (8 * hi + ((lane >> 2) & 3)) * 48 + ((lane & 16) ? 40 : 8 * (lane & 3));
                const unsigned xa = lds0 + L_XS + (a ? (l31 & 3) | ((l31 >> 1) & 4) : l31 & 7) * 1168 + hi * 16;
                f32x16 c = {};
                bf16x8 fw[5], fx[5];
                auto kv_read = [&](auto I) { fw[I.v % 5] = lds_tr2(ba + I.v * 768, ba + I.v * 768 + 192); fx[I.v % 5] = *(const LDS_AS bf16x8*)(size_t)(xa + I.v * 32); };
                cnt_wait<C_K, 0>(cb, 4);
                static_for<0, 4>(kv_read);
                if (a == 0) {
                    static_for<0, 36>([&](auto I) {
                        if constexpr (I.v + 4 < 36) kv_read(IC<I.v + 4>{});
                        SBAR(); c = MFMA32(fw[I.v % 5], fx[I.v % 5], c); SBAR();
                    });
                    PIN(c);
                    cnt_post<C_C>(cb, one);
                    const unsigned p0 = pack2(c[0], c[1]), p1 = pack2(c[2], c[3]), p2 = pack2(c[4], c[5]), p3 = pack2(c[6], c[7]), p4 = pack2(c[10], c[11]);
                    st16(u32x4{p0, p1, p2, p3}, rs, l31 < 8 ? hi * 512 + l31 * 16 : 0x80000000u, WS_K1 + (bid >> 2) * 1024 + (bid & 3) * 128);
                    __builtin_amdgcn_raw_buffer_store_b64(u32x2{p4, 0x3f80u}, rs, lane < 8 ? lane * 8 : 0x80000000u, WS_K2 + (bid >> 2) * 256 + (bid & 3) * 64, SC1);
                    float n2 = hi ? 0.f : blo(p4) * blo(p4) + bhi(p4) * bhi(p4);
                    n2 += blo(p0) * blo(p0) + bhi(p0) * bhi(p0) + blo(p1) * blo(p1) + bhi(p1) * bhi(p1);
                    n2 += blo(p2) * blo(p2) + bhi(p2) * bhi(p2) + blo(p3) * blo(p3) + bhi(p3) * bhi(p3);
                    const float kmax = half_max(sum32(n2));
                    waitn<0>();
                    flag_set(ws, WS_FKV + bid * 16, (u64)f2u(kmax) << 32 | (unsigned)tag, lane);
                } else {
                    static_for<0, 36>([&](auto I) {
                        if constexpr (I.v + 4 < 36) kv_read(IC<I.v + 4>{});
                        SBAR(); c = MFMA32(fx[I.v % 5], fw[I.v % 5], c); SBAR();
                    });
                    PIN(c);
                    cnt_post<C_C>(cb, one);
                    const u32x4 mv = {pack2(c[0], c[1]), pack2(c[2], c[3]), pack2(c[4], c[5]), pack2(c[6], c[7])};
                    st16(mv, rs, (lane < 16 || (lane & 62) == 18) ? (lane < 16 ? lane : lane - 2) * 16 : 0x80000000u, WS_V + bid * 288);
                    waitn<0>();
                    flag_set(ws, WS_FKV + bid * 16 + 8, tag, lane);
                }
            }
        }
        const f32x16 zero = {};
        bf16x8 q1[4];
        unsigned ql[4];
        float km;
        const char* kb = ws + WS_K1 + kh * 32 * 1024;
        const char* k2b = ws + WS_K2 + kh * 32 * 256;
        const char* vb = ws + WS_V + kh * 32 * 1152;
        unsigned vo8 = ((l31 >> 4) * 2 + ((l31 >> 2) & 1)) * 64 + (((l31 >> 3) & 1) * 4 + (l31 & 3)) * 8, vok = vo8 * 2 + hi * 512;
        unsigned vov = hi * 288 + l31 * 16;
        PIN(vo8); PIN(vok); PIN(vov);
        bf16x8 k1[4], v0[4], v1[4];
        s16x4 k2[4], q2[4];
#pragma unroll
        for (int i = 0; i < 4; ++i) { const unsigned cs = l31 == 18 ? 0x3f803f80u : 0u; v0[i] = v1[i] = __builtin_bit_cast(bf16x8, u32x4{cs, cs, cs, cs}); PIN(v0[i]); PIN(v1[i]); }
        {
            const char* fkv = ws + WS_FKV + kh * 2048;
            bf16x8 f0, f1;
            unsigned pf = 0;
            __builtin_amdgcn_s_setprio(0);
            const unsigned l17 = min(lane, 17);
            const unsigned vt1 = (2 * r_ + (a & 1)) * (FF * 2) + (l17 < 10 ? 4096 + 1280 * sp + l17 * 128 : 256 * FF * 2 + 1024 * sp + (l17 - 10) * 128);
            const unsigned vt1b = (2 * r_ + (a & 1)) * (FF * 2) + 4096 + 1280 * sp + min(lane, 9) * 128;
            const unsigned vt1a = (2 * r_ + (a & 1)) * (FF * 2) + 1024 * sp + min(lane, 7) * 128;
            const unsigned vt2 = (4 * r_ + a) * (DM * 2) + min(lane, 8) * 128;
            auto side = [&](int i) {
                cnt_wait<C_L, 8>(cb, 4 * (i + 1));
                if (PFB == 4 && i == 0) {
                    const unsigned idx = min((a & 1) * 64 + lane, 111);
                    touch(pf, (128 + 14 * r_ + (idx >> 3)) * (FF * 2) + 1024 * sp + (idx & 7) * 128, (a >> 1) ? w3 : w1);
                }
                if (PFB == 5) touch(pf, vt1b, ((a >> 1) ? w3 : w1) + (size_t)i * 64 * FF * 2);
                if (PFB == 4 && i >= 2) touch(pf, vt1b, ((a >> 1) ? w3 : w1) + (size_t)(i - 2) * 64 * FF * 2);
                if (PFB == 1) touch(pf, i < 5 ? vt1 : vt1b, ((a >> 1) ? w3 : w1) + (size_t)i * 64 * FF * 2);
                if ((PFB == 2 || PFB == 3) && i < 5) touch(pf, vt1a, ((a >> 1) ? w3 : w1) + (size_t)(i + 4) * 64 * FF * 2);
                if (PFB == 2 && i >= 4) touch(pf, vt1b, ((a >> 1) ? w3 : w1) + (size_t)(i - 4) * 64 * FF * 2);
            };
#pragma unroll
            for (int i = 0; i < KVW / 4 - 1; ++i) side(i);
            cnt_wait<C_L, 8>(cb, KVW);
            __builtin_amdgcn_s_setprio(3);
            ld16<0>(f0, vo16, fkv); ld16<1024>(f1, vo16, fkv);
            for (unsigned lim = SPIN_MAX; lim; --lim) {
                waitv<0>(f0, f1);
                const u32x4 a0 = __builtin_bit_cast(u32x4, f0), a1 = __builtin_bit_cast(u32x4, f1);
                const unsigned tl = (unsigned)tag, tu = (unsigned)(tag >> 32);
                if (all_lanes(a0[0] == tl && a0[2] == tl && a0[3] == tu && a1[0] == tl && a1[2] == tl && a1[3] == tu)) break;
                __builtin_amdgcn_s_sleep(16);
                ld16<0>(f0, vo16, fkv); ld16<1024>(f1, vo16, fkv);
            }
            waitv<0>(f0, f1);
            km = fmaxf(u2f(__builtin_bit_cast(u32x4, f0)[1]), u2f(__builtin_bit_cast(u32x4, f1)[1]));
            ld16<0>(k1[0], vok, kb); ld8<0>(k2[0], vo8, k2b);
            static_for<0, 3>([&](auto B) {
                ld16<(B.v + 1) * 1024>(k1[B.v + 1], vok, kb); ld8<(B.v + 1) * 256>(k2[B.v + 1], vo8, k2b);
                ldv(v0[B.v], v1[B.v], vov, vb + B.v * 1152);
            });
            km = max32(half_max(km));
            PIN(km);
            __builtin_amdgcn_s_setprio(0);
#pragma unroll
            for (int i = KVW / 4 - 1; i < 9; ++i) side(i);
            if (PFG) touch(pf, l17 < 9 ? vt2 : (128 + ((4 * r_ + a) & 15)) * (DM * 2) + (l17 - 9) * 128, wo + (size_t)144 * sp * (DM * 2));
            cnt_wait<C_L, 8>(cb, 40);
            __builtin_amdgcn_s_setprio(3);
            cnt_wait<C_Q, 1>(cb + 4 * par, 2);
#pragma unroll
            for (int qb = 0; qb < 4; ++qb) {
                const unsigned src = lds0 + L_Q + (4 * par + qb) * 1280;
                q1[qb] = *(const LDS_AS bf16x8*)(size_t)(src + vo16);
                ql[qb] = *(const LDS_AS unsigned*)(size_t)(src + 1024 + l31 * 4);
            }
            asm volatile("s_waitcnt lgkmcnt(0)" ::: "memory");
#pragma unroll
            for (int qb = 0; qb < 4; ++qb) { PIN(q1[qb]); PIN(ql[qb]); }
            cnt_post<C_A>(cb, one);
            waitv<0>(pf);
        }
        float mrow[4];
        bool safe = true;
#pragma unroll
        for (int qb = 0; qb < 4; ++qb) {
            const u32x4 u = __builtin_bit_cast(u32x4, q1[qb]);
            float n2 = hi ? 0.f : blo(ql[qb]) * blo(ql[qb]) + bhi(ql[qb]) * bhi(ql[qb]);
#pragma unroll
            for (int i = 0; i < 4; ++i) n2 += blo(u[i]) * blo(u[i]) + bhi(u[i]) * bhi(u[i]);
            const float bound = bf16r(__builtin_amdgcn_sqrtf(sum32(n2) * km) * 1.01f);
            safe = safe && (bound <= SM_SAFE);
            mrow[qb] = bound;
            q2[qb] = __builtin_bit_cast(s16x4, u32x2{hi ? 0u : ql[qb], hi ? 0u : pack2(-bound, 0.f)});
        }
        f32x16 o[4];
#pragma unroll
        for (int qb = 0; qb < 4; ++qb) o[qb] = f32x16{};
        asm volatile("s_waitcnt vmcnt(0)" : "+v"(k1[0]), "+v"(k1[1]), "+v"(k1[2]), "+v"(k1[3]), "+v"(k2[0]), "+v"(k2[1]), "+v"(k2[2]), "+v"(k2[3]),
                     "+v"(v0[0]), "+v"(v0[1]), "+v"(v0[2]), "+v"(v1[0]), "+v"(v1[1]), "+v"(v1[2]) :: "memory");
        __builtin_amdgcn_s_setprio(LOWT ? 0 : APRIO);
        if (all_lanes(safe)) {
            f32x16 s[2];
            unsigned p0[4], p1[4] = {0u, 0u, 0u, 0u};
            s[0] = MFMA32(k1[0], q1[0], zero); s[0] = MFMA8(k2[0], q2[0], s[0]);
            SBAR();
            for (int t4 = 0; t4 < 32; t4 += 4) {
                static_for<0, 16>([&](auto UU) {
                    constexpr int b = UU.v >> 2, qb = UU.v & 3, bn = (qb == 3) ? (b + 1) & 3 : b, bp = (qb == 0) ? (b + 3) & 3 : b;
                    f32x16& c = s[qb & 1];
                    f32x16& nx = s[(qb + 1) & 1];
                    float e0, e1, e2, e3;
#define EXP2(a, b, i) a = __builtin_amdgcn_exp2f(c[i]); b = __builtin_amdgcn_exp2f(c[i + 1]); PIN(a); PIN(b)
#define CVT2(p, i) p[i] = pack2(e0, e1); p[i + 1] = pack2(e2, e3); PIN(p[i]); PIN(p[i + 1])
                    if constexpr (LOWT && qb == 0 && b == (LOWT & 3)) { if (t4 == (LOWT & ~3)) __builtin_amdgcn_s_setprio(APRIO); }
                    EXP2(e0, e1, 0);
                    if constexpr (qb == 3) waitv<10>(k1[bn], k2[bn]);
                    SBAR();
                    nx = MFMA32(k1[bn], q1[(qb + 1) & 3], zero);
                    SBAR();
                    EXP2(e2, e3, 2); CVT2(p0, 0); EXP2(e0, e1, 4);
                    SBAR();
                    nx = MFMA8(k2[bn], q2[(qb + 1) & 3], nx);
                    SBAR();
                    if constexpr (qb == 3) {
                        const int tn = min(t4 + b + 4, 31);
                        ld16<0>(k1[b], vok, kb + tn * 1024); ld8<0>(k2[b], vo8, k2b + tn * 256);
                    }
                    EXP2(e2, e3, 6); CVT2(p0, 2); EXP2(e0, e1, 8);
                    SBAR();
                    o[(qb + 3) & 3] = MFMA32(v1[bp], __builtin_bit_cast(bf16x8, u32x4{p1[0], p1[1], p1[2], p1[3]}), o[(qb + 3) & 3]);
                    SBAR();
                    if constexpr (qb == 1) {
                        ldv(v0[(b + 3) & 3], v1[(b + 3) & 3], vov, vb + min(t4 + b + 3, 31) * 1152);
                    }
                    EXP2(e2, e3, 10); CVT2(p1, 0); EXP2(e0, e1, 12);
                    if constexpr (qb == 0) waitv<10>(v0[b], v1[b]);
                    SBAR();
                    o[qb] = MFMA32(v0[b], __builtin_bit_cast(bf16x8, u32x4{p0[0], p0[1], p0[2], p0[3]}), o[qb]);
                    SBAR();
                    EXP2(e2, e3, 14); CVT2(p1, 2);
                    SBAR();
                });
            }
            o[3] = MFMA32(v1[3], __builtin_bit_cast(bf16x8, u32x4{p1[0], p1[1], p1[2], p1[3]}), o[3]);
            asm volatile("s_waitcnt vmcnt(0)" : "+v"(k1[0]), "+v"(k1[1]), "+v"(k1[2]), "+v"(k1[3]), "+v"(k2[0]), "+v"(k2[1]), "+v"(k2[2]), "+v"(k2[3]),
                         "+v"(v0[0]), "+v"(v0[1]), "+v"(v0[2]), "+v"(v1[0]), "+v"(v1[1]), "+v"(v1[2]) :: "memory");
        } else {
#pragma unroll
            for (int qb = 0; qb < 4; ++qb) { q2[qb][2] = 0; mrow[qb] = -3.0e38f; }
#pragma unroll 1
            for (int t = 0; t < 32; ++t) {
                ld16<0>(k1[0], vok, kb + t * 1024); ld8<0>(k2[0], vo8, k2b + t * 256);
                ldv(v0[0], v1[0], vov, vb + t * 1152);
                waitv<0>(k1[0], k2[0]); waitv<0>(v0[0], v1[0]);
#pragma unroll
                for (int qb = 0; qb < 4; ++qb) {
                    f32x16 c = MFMA32(k1[0], q1[qb], zero);
                    c = MFMA8(k2[0], q2[qb], c);
                    float mx = c[0];
#pragma unroll
                    for (int r = 1; r < 16; ++r) mx = fmaxf(mx, c[r]);
                    const float mn = fmaxf(mrow[qb], max32(mx)), al = __builtin_amdgcn_exp2f(mrow[qb] - mn);
                    mrow[qb] = mn;
                    u32x4 p0, p1;
#pragma unroll
                    for (int r = 0; r < 4; ++r) {
                        p0[r] = pack2(__builtin_amdgcn_exp2f(c[2 * r] - mn), __builtin_amdgcn_exp2f(c[2 * r + 1] - mn));
                        p1[r] = pack2(__builtin_amdgcn_exp2f(c[8 + 2 * r] - mn), __builtin_amdgcn_exp2f(c[9 + 2 * r] - mn));
                    }
#pragma unroll
                    for (int r = 0; r < 16; ++r) o[qb][r] *= al;
                    o[qb] = MFMA32(v0[0], __builtin_bit_cast(bf16x8, p0), o[qb]);
                    o[qb] = MFMA32(v1[0], __builtin_bit_cast(bf16x8, p1), o[qb]);
                }
            }
        }
        __builtin_amdgcn_s_setprio(MPRIO);
        const unsigned ex = lds0 + L_EX + vo16;
#pragma unroll
        for (int j = 0; j < 2; ++j) {
            const f32x16& s = kh ? o[2 * j] : o[2 * j + 1];
            const float mm = kh ? mrow[2 * j] : mrow[2 * j + 1];
            lds_st4(ex + a * 6144 + j * 3072, s[0], s[1], s[2], s[3]);
            lds_st4(ex + a * 6144 + j * 3072 + 1024, s[4], s[5], s[6], s[7]);
            lds_st4(ex + a * 6144 + j * 3072 + 2048, s[8], s[9], s[10], mm);
        }
        asm volatile("s_waitcnt lgkmcnt(0)" ::: "memory");
        cnt_post<C_X>(cb + 4 * par, one);
        cnt_wait<C_X, 0>(cb + 4 * par, 2);
#pragma unroll
        for (int j = 0; j < 2; ++j) {
            const f32x16& s = kh ? o[2 * j + 1] : o[2 * j];
            const float ma = kh ? mrow[2 * j + 1] : mrow[2 * j];
            const unsigned src = ex + (a ^ 2) * 6144 + j * 3072;
            const f32x4 t0 = lds_f4(src), t1 = lds_f4(src + 1024), t2 = lds_f4(src + 2048);
            const float mb = t2[3], mm = fmaxf(ma, mb);
            const float fa = __builtin_amdgcn_exp2f(ma - mm), fb = __builtin_amdgcn_exp2f(mb - mm);
            const float inv = __builtin_amdgcn_rcpf(low32(s[10] * fa + t2[2] * fb)), ia = fa * inv, ib = fb * inv;
            const u32x4 pk = {pack2(s[0] * ia + t0[0] * ib, s[1] * ia + t0[1] * ib), pack2(s[2] * ia + t0[2] * ib, s[3] * ia + t0[3] * ib),
                              pack2(s[4] * ia + t1[0] * ib, s[5] * ia + t1[1] * ib), pack2(s[6] * ia + t1[2] * ib, s[7] * ia + t1[3] * ib)};
            const int tb = 8 * mt + 4 * par + 2 * j + kh;
            st16(pk, rs, vo16, WS_O + (tb * 36 + nt) * 1024);
            __builtin_amdgcn_raw_buffer_store_b32(pack2(s[8] * ia + t2[0] * ib, s[9] * ia + t2[1] * ib), rs, hi ? 0x80000000u : vo16,
                                                  WS_O + (tb * 36 + 32 + sp) * 1024 + (nt & 4) * 128 + (nt & 1) * 8 + (nt & 2) * 2, SC1);
        }
        waitn<0>();
        flag_set(ws, WS_FO + ((mt * 32 + nt) * 4 + a) * FS, tag, lane);
        __builtin_amdgcn_s_setprio(0);
        const size_t orow = (size_t)((4 * mt2 + sp) * 32 + l31) * (DM * 2) + nt2 * 288 + hi * 16;
        u32x4 xr[3];
#pragma unroll
        for (int i = 0; i < 3; ++i) xr[i] = *(const u32x4*)(x + orow + (i < 2 ? a + 4 * i : 8) * 32);
        if (bid == 0 && a == 1) {
#pragma unroll
            for (int r = 0; r < 4; ++r) flag_wait(ws, WS_FX + (r * 64 + lane) * FS, tag);
            flag_set(ws, WS_EPOCH, tag - TAG_STEP + 1, lane);
        }
        cnt_wait<C_P, 4>(cb, 1);
        f32x4 s0[3], s1[3];
#pragma unroll
        for (int i = 0; i < 3; ++i) {
            const unsigned src = lds0 + G_Y + (i < 2 ? a + 4 * i : 8) * 2048 + lane * 32;
            s0[i] = lds_f4(src) + f32x4{blo(xr[i][0]), bhi(xr[i][0]), blo(xr[i][1]), bhi(xr[i][1])};
            s1[i] = lds_f4(src + 16) + f32x4{blo(xr[i][2]), bhi(xr[i][2]), blo(xr[i][3]), bhi(xr[i][3])};
            PIN(s0[i]); PIN(s1[i]);
        }
        if (P2 & 2) flag_wait2<12>(ws, WS_FP + (bid * 4 + ((sp + 1 + min(lane, 2)) & 3)) * FPS, tag);
        else flag_wait(ws, WS_FP + (bid * 4 + ((sp + 1 + min(lane, 2)) & 3)) * FPS, tag);
        u32x4 pp[3][3];
#pragma unroll
        for (int r = 0; r < 3; ++r)
#pragma unroll
            for (int i = 0; i < 3; ++i)
                pp[r][i] = __builtin_amdgcn_raw_buffer_load_b128(rs, (i < 2 || own8) ? vo16 : 0x80000000u,
                                                                 WS_P + (bid * 4 + ((sp + 1 + r) & 3)) * 9216 + (i < 2 ? a + 4 * i : 8) * 1024, SC1);
        SBAR();
#pragma unroll
        for (int i = 0; i < 3; ++i) {
#pragma unroll
            for (int r = 0; r < 3; ++r) {
                s0[i] += f32x4{blo(pp[r][i][0]), bhi(pp[r][i][0]), blo(pp[r][i][1]), bhi(pp[r][i][1])};
                s1[i] += f32x4{blo(pp[r][i][2]), bhi(pp[r][i][2]), blo(pp[r][i][3]), bhi(pp[r][i][3])};
            }
            if (i < 2 || own8)
                *(u32x4*)(out + orow + (i < 2 ? a + 4 * i : 8) * 32) = u32x4{pack2(s0[i][0], s0[i][1]), pack2(s0[i][2], s0[i][3]), pack2(s1[i][0], s1[i][1]), pack2(s1[i][2], s1[i][3])};
        }
    }
}

#ifdef __HIP_DEVICE_COMPILE__
#pragma clang attribute pop
#endif

extern "C" void kernel_launch(void* const* d_in, const int* in_sizes, int n_in, void* d_out, int out_size, void* d_ws, size_t ws_size,
                              hipStream_t stream) {
    (void)hipFuncSetAttribute((const void*)k_all, hipFuncAttributeMaxDynamicSharedMemorySize, LDS_BYTES);
    k_all<<<256, 512, LDS_BYTES, stream>>>((const char*)d_in[0], (const char*)d_in[1], (const char*)d_in[2], (const char*)d_in[3],
                                           (const char*)d_in[4], (const char*)d_in[6], (const char*)d_in[7], (const char*)d_in[8],
                                           (char*)d_ws, (char*)d_out);
}
```

```cpp
#include <hip/hip_runtime.h>
#include <stdint.h>

#ifdef __HIP_DEVICE_COMPILE__
#pragma clang attribute push(__attribute__((target("no-packed-fp32-ops"))), apply_to = function)
#endif

typedef __attribute__((ext_vector_type(8))) __bf16 bf16x8;
typedef __attribute__((ext_vector_type(2))) __bf16 bf16x2;
typedef __attribute__((ext_vector_type(4))) short s16x4;
typedef __attribute__((ext_vector_type(8))) short s16x8;
typedef __attribute__((ext_vector_type(4))) float f32x4;
typedef __attribute__((ext_vector_type(2))) float f32x2;
typedef __attribute__((ext_vector_type(16))) float f32x16;
typedef __attribute__((ext_vector_type(4))) unsigned u32x4;
typedef __attribute__((ext_vector_type(2))) unsigned u32x2;
typedef unsigned long long u64;
#define LDS_AS __attribute__((address_space(3)))
#define GLB_AS __attribute__((address_space(1)))

typedef __attribute__((ext_vector_type(3))) unsigned u32x3;

constexpr int DM = 576, FF = 4608;
constexpr unsigned FS = 64;
constexpr unsigned WS_EPOCH = 0, WS_FKV = 4096, WS_FX = 8192, WS_FO = WS_FX + 256 * FS, WS_FH = WS_FO + 1024 * FS, WS_FP = WS_FH + 2048 * FS;
constexpr int FPS = 128;
constexpr unsigned WS_XN = WS_FP + 1024 * 128;
constexpr unsigned WS_H = WS_XN + 64u * 36 * 1024;
constexpr unsigned WS_K1 = WS_H + 64u * 288 * 1024;
constexpr unsigned WS_K2 = WS_K1 + 64u * 1024;
constexpr unsigned WS_V = WS_K2 + 64u * 256;
constexpr unsigned WS_O = WS_V + 64u * 2048;
constexpr unsigned WS_P = WS_O + 64u * 36 * 1024;
constexpr unsigned WS_END = WS_P + 1024u * 9216;

constexpr int W_PART = 10368, W_SLOT = 2 * W_PART;
constexpr int L_KV = 3 * W_SLOT, L_WQ = L_KV + 2 * 27648;
constexpr int L_Q = L_KV + 4096, L_XS = 2 * W_SLOT;
constexpr int G_X = 64512, G_Y = G_X + 36864, G_Z = 0, G_L = 36864;
constexpr int L_EX = G_Y + 36864;
constexpr int L_CNT = L_EX + 24576;
constexpr int C_L = 0, C_C = 4, C_A = 8, C_S = 12, C_F = 16, C_B = 20, C_Q = 28, C_X = 36, C_P = 44, C_K = 24, C_TAG = 56;
constexpr int LDS_BYTES = 163840;
typedef char lds_map_check_t[(L_WQ + 27648 <= L_CNT && L_Q >= G_X && 3 * W_SLOT <= G_X && L_CNT + 1024 <= LDS_BYTES && G_L + 9216 <= G_X) ? 1 : -1];

constexpr float SM_SAFE = 48.f;
constexpr int APRIO = 2;
constexpr unsigned SPIN_MAX = 1u << 21;
constexpr int GPRIO = 0;
constexpr int LOWT = 0;
constexpr int JSB = 12;
constexpr int KVW = 12;
constexpr int EPA = 2;
constexpr int CPF = 23;
constexpr int PFG = 9;
constexpr int OD = 8;
constexpr int MPRIO = 3;
constexpr u64 TAG_STEP = 0x9e3779b97f4a7c15ull;

#define SBAR() __builtin_amdgcn_sched_barrier(0)
#define MFMA32(a, b, c) __builtin_amdgcn_mfma_f32_32x32x16_bf16(a, b, c, 0, 0, 0)
#define MFMA8(a, b, c) __builtin_amdgcn_mfma_f32_32x32x8bf16_1k(a, b, c, 0, 0, 0)
#define PIN(x) asm volatile("" : "+v"(x))
#define SC1 16

__device__ __forceinline__ float u2f(unsigned u) { return __builtin_bit_cast(float, u); }
__device__ __forceinline__ unsigned f2u(float f) { return __builtin_bit_cast(unsigned, f); }
__device__ __forceinline__ bool all_lanes(bool p) { return __builtin_amdgcn_ballot_w64(!p) == 0; }
template <int N> struct IC { static constexpr int v = N; };
template <int I, int N, class F> __device__ __forceinline__ void static_for(F&& f) {
    if constexpr (I < N) { f(IC<I>{}); static_for<I + 1, N>(f); }
}

__device__ __forceinline__ void set_m0(unsigned lds_base) { asm volatile("s_mov_b32 m0, %0\n\ts_nop 0" :: "s"(lds_base) : "memory"); }
template <int OFF> __device__ __forceinline__ void glds(unsigned voff, const char* sbase) {
    asm volatile("global_load_lds_dwordx4 %0, %1 offset:%2" :: "v"(voff), "s"(sbase), "n"(OFF) : "memory");
}
template <int OFF> __device__ __forceinline__ void ld16(bf16x8& d, unsigned voff, const char* sbase) {
    asm volatile("global_load_dwordx4 %0, %1, %2 offset:%3 sc1" : "=v"(d) : "v"(voff), "s"(sbase), "n"(OFF) : "memory");
}
template <int OFF> __device__ __forceinline__ void ld8(s16x4& d, unsigned voff, const char* sbase) {
    asm volatile("global_load_dwordx2 %0, %1, %2 offset:%3 sc1" : "=v"(d) : "v"(voff), "s"(sbase), "n"(OFF) : "memory");
}
__device__ __forceinline__ void ldv(bf16x8& a, bf16x8& b, unsigned voff, const char* sbase) {
    asm volatile("s_mov_b64 exec, %4\n\tglobal_load_dwordx4 %0, %2, %3 sc1\n\tglobal_load_dwordx4 %1, %2, %3 offset:576 sc1\n\ts_mov_b64 exec, -1"
                 : "+v"(a), "+v"(b) : "v"(voff), "s"(sbase), "s"(0x0003ffff0003ffffull) : "memory");
}
__device__ __forceinline__ void touch(unsigned& d, unsigned voff, const char* sbase) {
    asm volatile("global_load_dword %0, %1, %2" : "+v"(d) : "v"(voff), "s"(sbase) : "memory");
}
__device__ __forceinline__ void ldflag(u64& d, unsigned voff, const char* sbase) {
    asm volatile("global_load_dwordx2 %0, %1, %2 sc1" : "=v"(d) : "v"(voff), "s"(sbase) : "memory");
}
template <int N, class T> __device__ __forceinline__ void waitv(T& a) { asm volatile("s_waitcnt vmcnt(%1)" : "+v"(a) : "n"(N < 63 ? N : 63) : "memory"); }
template <int N, class T, class U> __device__ __forceinline__ void waitv(T& a, U& b) {
    asm volatile("s_waitcnt vmcnt(%2)" : "+v"(a), "+v"(b) : "n"(N < 63 ? N : 63) : "memory");
}
template <class T> __device__ __forceinline__ void pin(T& a) { asm volatile("" : "+v"(a) :: "memory"); }
template <int N> __device__ __forceinline__ void waitn() { asm volatile("s_waitcnt vmcnt(%0)" :: "n"(N < 63 ? N : 63) : "memory"); }

__device__ __forceinline__ void st16(u32x4 v, __amdgpu_buffer_rsrc_t rs, unsigned voff, unsigned soff) {
    __builtin_amdgcn_raw_buffer_store_b128(v, rs, voff, soff, SC1);
    asm volatile("s_nop 1" :: "v"(v) : "memory");
}
__device__ __forceinline__ bf16x8 lds_tr2(unsigned a0, unsigned a1) {
    s16x4 lo = __builtin_amdgcn_ds_read_tr16_b64_v4i16((LDS_AS s16x4*)(size_t)a0);
    s16x4 hi = __builtin_amdgcn_ds_read_tr16_b64_v4i16((LDS_AS s16x4*)(size_t)a1);
    s16x8 r = __builtin_shufflevector(lo, hi, 0, 1, 2, 3, 4, 5, 6, 7);
    return __builtin_bit_cast(bf16x8, r);
}
__device__ __forceinline__ f32x4 lds_f4(unsigned a) { return *(const LDS_AS f32x4*)(size_t)a; }
__device__ __forceinline__ void lds_st4(unsigned a, float v0, float v1, float v2, float v3) { *(LDS_AS f32x4*)(size_t)a = f32x4{v0, v1, v2, v3}; }
__device__ __forceinline__ unsigned pack2(float lo, float hi) {
    bf16x2 v = {(__bf16)lo, (__bf16)hi};
    return __builtin_bit_cast(unsigned, v);
}
__device__ __forceinline__ float bf16r(float v) { return (float)(__bf16)v; }
__device__ __forceinline__ float blo(unsigned u) { return u2f(u << 16); }
__device__ __forceinline__ float bhi(unsigned u) { return u2f(u & 0xffff0000u); }
__device__ __forceinline__ float sum32(float a) {
    auto r = __builtin_amdgcn_permlane32_swap(f2u(a), f2u(a), false, false);
    return u2f(r[0]) + u2f(r[1]);
}
__device__ __forceinline__ float max32(float a) {
    auto r = __builtin_amdgcn_permlane32_swap(f2u(a), f2u(a), false, false);
    return fmaxf(u2f(r[0]), u2f(r[1]));
}
__device__ __forceinline__ float low32(float a) {
    auto r = __builtin_amdgcn_permlane32_swap(f2u(a), f2u(a), false, false);
    return u2f(r[0]);
}
__device__ __forceinline__ u64 uni64(u64 v) {
    return (u64)(unsigned)__builtin_amdgcn_readfirstlane((unsigned)(v >> 32)) << 32 | (unsigned)__builtin_amdgcn_readfirstlane((unsigned)v);
}
template <int CTRL> __device__ __forceinline__ float dpp(float a) { return u2f(__builtin_amdgcn_mov_dpp(f2u(a), CTRL, 0xf, 0xf, true)); }
__device__ __forceinline__ float half_sum(float a) {
    a += dpp<0xB1>(a); a += dpp<0x4E>(a); a += dpp<0x141>(a); a += dpp<0x140>(a);
    auto r = __builtin_amdgcn_permlane16_swap(f2u(a), f2u(a), false, false);
    return u2f(r[0]) + u2f(r[1]);
}
__device__ __forceinline__ float half_max(float a) {
    a = fmaxf(a, dpp<0xB1>(a)); a = fmaxf(a, dpp<0x4E>(a)); a = fmaxf(a, dpp<0x141>(a)); a = fmaxf(a, dpp<0x140>(a));
    auto r = __builtin_amdgcn_permlane16_swap(f2u(a), f2u(a), false, false);
    return fmaxf(u2f(r[0]), u2f(r[1]));
}
__device__ __forceinline__ unsigned cnt_add(unsigned addr) {
    unsigned old, r;
    asm volatile("s_mov_b64 exec, 1\n\tds_add_rtn_u32 %0, %2, %3\n\ts_waitcnt lgkmcnt(0)\n\tv_readfirstlane_b32 %1, %0\n\ts_mov_b64 exec, -1"
                 : "=&v"(old), "=s"(r) : "v"(addr), "v"(1u) : "memory");
    return r;
}
template <int OFF> __device__ __forceinline__ void cnt_post(unsigned base, unsigned one) {
    asm volatile("s_mov_b64 exec, 1\n\tds_add_u32 %0, %1 offset:%2\n\ts_mov_b64 exec, -1" :: "v"(base), "v"(one), "n"(OFF) : "memory");
}
template <int OFF, int SLEEP = 1> __device__ __forceinline__ void cnt_wait(unsigned base, unsigned target) {
    unsigned v, t, lim = SPIN_MAX * 8;
    asm volatile("1:\n\tds_read_b32 %0, %3 offset:%5\n\ts_waitcnt lgkmcnt(0)\n\tv_readfirstlane_b32 %1, %0\n\ts_cmp_ge_u32 %1, %4\n\ts_cbranch_scc1 2f\n\t"
                 "s_sleep %6\n\ts_sub_u32 %2, %2, 1\n\ts_cbranch_scc0 1b\n2:"
                 : "=&v"(v), "=&s"(t), "+s"(lim) : "v"(base), "s"(target), "n"(OFF), "n"(SLEEP) : "memory", "scc");
}
template <int OFF> __device__ __forceinline__ unsigned cnt_peek(unsigned base) { return *(volatile const LDS_AS unsigned*)(size_t)(base + OFF); }
template <int OFF, int TARGET> __device__ __forceinline__ void cnt_check(unsigned base, unsigned v) {
    unsigned t, lim;
    asm volatile("s_setprio 3\n\tv_readfirstlane_b32 %1, %0\n\ts_setprio %7\n\ts_cmp_ge_u32 %1, %4\n\ts_cbranch_scc1 2f\n\ts_mov_b32 %2, %6\n"
                 "1:\n\tds_read_b32 %0, %3 offset:%5\n\ts_waitcnt lgkmcnt(0)\n\tv_readfirstlane_b32 %1, %0\n\ts_cmp_ge_u32 %1, %4\n\ts_cbranch_scc1 2f\n\t"
                 "s_sub_u32 %2, %2, 1\n\ts_cbranch_scc0 1b\n2:"
                 : "+v"(v), "=&s"(t), "=&s"(lim) : "v"(base), "n"(TARGET), "n"(OFF), "n"(SPIN_MAX * 8), "n"(GPRIO) : "memory", "scc");
}
template <int OFF> __device__ __forceinline__ unsigned add_now(unsigned base) {
    unsigned r;
    asm volatile("s_setprio 3\n\tv_add_u32 %0, %2, %1\n\ts_setprio %3" : "=v"(r) : "v"(base), "n"(OFF), "n"(GPRIO));
    return r;
}
__device__ __forceinline__ void flag_set(char* ws, unsigned off, u64 tag, int lane) {
    if (lane == 0) __hip_atomic_store((GLB_AS u64*)(ws + off), tag, __ATOMIC_RELAXED, __HIP_MEMORY_SCOPE_AGENT);
}
__device__ __forceinline__ void flag_put(const char* ws, unsigned off, u64 tag) {
    unsigned o; u64 t;
    asm volatile("s_mov_b64 exec, 1\n\ts_setprio 3\n\tv_mov_b32 %0, %2\n\tv_mov_b64 %1, %3\n\ts_setprio %5\n\tglobal_store_dwordx2 %0, %1, %4 sc1\n\ts_mov_b64 exec, -1"
                 : "=&v"(o), "=&v"(t) : "s"(off), "s"(tag), "s"(ws), "n"(GPRIO) : "memory");
}
__device__ __forceinline__ void flag_wait(const char* ws, unsigned off, u64 tag) {
    u64 v; unsigned lim = SPIN_MAX;
    asm volatile("1:\n\tglobal_load_dwordx2 %0, %2, %3 sc1\n\ts_waitcnt vmcnt(0)\n\tv_cmp_ne_u64 vcc, %4, %0\n\ts_cmp_eq_u64 vcc, 0\n\ts_cbranch_scc1 2f\n\t"
                 "s_sleep 2\n\ts_sub_u32 %1, %1, 1\n\ts_cbranch_scc0 1b\n2:"
                 : "=&v"(v), "+s"(lim) : "v"(off), "s"(ws), "s"(tag) : "memory", "scc", "vcc");
}
__device__ __forceinline__ void flag_check(const char* ws, unsigned off, u64 tag, u64 v) {
    unsigned lim;
    asm volatile("s_setprio 3\n\tv_cmp_ne_u64 vcc, %4, %0\n\ts_setprio %6\n\ts_cmp_eq_u64 vcc, 0\n\ts_cbranch_scc1 2f\n\ts_mov_b32 %1, %5\n"
                 "1:\n\tglobal_load_dwordx2 %0, %2, %3 sc1\n\ts_waitcnt vmcnt(0)\n\tv_cmp_ne_u64 vcc, %4, %0\n\ts_cmp_eq_u64 vcc, 0\n\ts_cbranch_scc1 2f\n\t"
                 "s_sleep 1\n\ts_sub_u32 %1, %1, 1\n\ts_cbranch_scc0 1b\n2:"
                 : "+v"(v), "=&s"(lim) : "v"(off), "s"(ws), "s"(tag), "n"(SPIN_MAX), "n"(GPRIO) : "memory", "scc", "vcc");
}
__device__ __forceinline__ void swiglu4(unsigned& o0, unsigned& o1, float g0, float g1, float g2, float g3, float u0, float u1, float u2, float u3) {
    float t0, t1, t2, t3, p0, p1, p2, p3;
    asm("v_mul_f32 %2, 0xbfb8aa3b, %10\n\tv_mul_f32 %3, 0xbfb8aa3b, %11\n\tv_mul_f32 %4, 0xbfb8aa3b, %12\n\tv_mul_f32 %5, 0xbfb8aa3b, %13\n\t"
        "v_exp_f32 %2, %2\n\tv_exp_f32 %3, %3\n\tv_exp_f32 %4, %4\n\tv_exp_f32 %5, %5\n\t"
        "v_mul_f32 %6, %10, %14\n\tv_mul_f32 %7, %11, %15\n\tv_mul_f32 %8, %12, %16\n\tv_mul_f32 %9, %13, %17\n\t"
        "v_add_f32 %2, 1.0, %2\n\tv_add_f32 %3, 1.0, %3\n\tv_add_f32 %4, 1.0, %4\n\tv_add_f32 %5, 1.0, %5\n\t"
        "v_rcp_f32 %2, %2\n\tv_rcp_f32 %3, %3\n\tv_rcp_f32 %4, %4\n\tv_rcp_f32 %5, %5\n\t"
        "v_mul_f32 %6, %6, %2\n\tv_mul_f32 %7, %7, %3\n\tv_mul_f32 %8, %8, %4\n\tv_mul_f32 %9, %9, %5\n\t"
        "v_cvt_pk_bf16_f32 %0, %6, %7\n\tv_cvt_pk_bf16_f32 %1, %8, %9"
        : "=v"(o0), "=v"(o1), "=&v"(t0), "=&v"(t1), "=&v"(t2), "=&v"(t3), "=&v"(p0), "=&v"(p1), "=&v"(p2), "=&v"(p3)
        : "v"(g0), "v"(g1), "v"(g2), "v"(g3), "v"(u0), "v"(u1), "v"(u2), "v"(u3));
}

__device__ __forceinline__ int kperm(int r) { return (r & 8) + ((r & 1) << 2) + ((r >> 1) & 3); }

constexpr int S_FA = 42, S_SA = 60, S_CA = 66;
constexpr int g1_piece(int s, int u) { const int U = (s & 3) * 5 + u; return (U >= 2 && U <= ((s >> 2) <= 15 ? 6 : 10)) || (U == 13 && (s >> 2) >= PFG) ? 1 : 0; }
constexpr int g1_cum(int s, int u) {
    int n = 0;
    for (int t = 0; t <= s; ++t) {
        const int e = t < s ? 5 : u;
        if (e > 0 && t == S_FA) ++n;
        for (int i = 0; i < e; ++i) n += g1_piece(t, i);
        if (t < s) n += 2 + (t == S_SA ? 1 : 0) + (t == 35 ? 8 : 0);
    }
    return n;
}
constexpr int g1_upto_loads(int s) { return g1_cum(s + 1, 0) - (s == 35 ? 8 : 0); }
constexpr int g1_upto_unit(int T, int U) { return g1_cum(4 * T + U / 5, U % 5 + 1); }
constexpr int J_FB = 3, J_SB = JSB, J_CB = 16, J_CO = 72 - OD, J_SO = J_CO - 5, HR = 16;
constexpr int G2_BASE = g1_cum(72, 0) + 10 + (HR - 12);
constexpr int g2_pieces(int J) { const int j = J & 7; return (J < 64 ? (j >= 1 && j <= 4 ? 2 : j == 5 ? 1 : 0) : 0) + (J == 6 ? 2 : 0); }
constexpr int g2_next(int J) { return J < 72 - HR ? J + HR : (J >= 72 - OD && J <= 80 - OD) ? J + OD : -1; }
constexpr int g2_misc(int J) { return (J == J_FB || J == J_SB || J == J_SO) ? 1 : 0; }
constexpr int g2_cum(int J) { int n = G2_BASE; for (int t = 0; t < J; ++t) n += g2_pieces(t) + g2_misc(t) + (g2_next(t) >= 0 ? 1 : 0); return n; }
constexpr int g2_upto_frag(int J) {
    if (J < 12) return g1_upto_loads(66 + J / 2) - ((J & 1) ? 0 : 1);
    if (J < HR) return G2_BASE - (HR - 1 - J);
    return g2_cum((J < 72 ? J - HR : J - OD) + 1);
}
constexpr int g2_slot(int T) { return T % 3 == 0 ? G_X : T % 3 == 1 ? G_Y : G_Z; }

__global__ __launch_bounds__(512, 2) void k_all(const char* __restrict__ x, const char* __restrict__ wq, const char* __restrict__ wk,
                                                const char* __restrict__ wv, const char* __restrict__ wo, const char* __restrict__ w1,
                                                const char* __restrict__ w3, const char* __restrict__ w2, char* ws, char* __restrict__ out) {
    extern __shared__ __attribute__((aligned(16))) char smem[];
    u64 pc;
    asm volatile("s_getpc_b64 %0" : "=s"(pc));
    const unsigned lds0 = (unsigned)(size_t)(LDS_AS char*)smem;
    const int lane = threadIdx.x & 63, w = __builtin_amdgcn_readfirstlane(threadIdx.x >> 6);
    const int bid = blockIdx.x;
    const int l31 = lane & 31, hi = lane >> 5;
    const __amdgpu_buffer_rsrc_t rs = __builtin_amdgcn_make_buffer_rsrc(ws, 0, WS_END, 0x00020000);
    const int th = bid & 1, sp = (bid >> 1) & 3, r_ = bid >> 3;
    const int mt = 4 * th + (r_ >> 3), nt = 8 * sp + (r_ & 7);
    const int mt2 = 8 * th + (r_ >> 2), nt2 = r_ & 3;
    unsigned pc0;
    {
        const unsigned vo = (r_ * CPF + min(lane, CPF - 1)) * 128;
        asm volatile("s_cmp_lg_u32 %3, 0\n\ts_cbranch_scc1 1f\n\tglobal_load_dword %0, %1, %2\n1:" : "=v"(pc0) : "v"(vo), "s"(pc), "s"(w) : "memory", "scc");
    }
    unsigned cb = lds0 + L_CNT, one = 1u, vo16 = lane * 16;
    PIN(cb); PIN(one); PIN(vo16);
    auto q_groups = [&](int g0, int n) {
        unsigned vi[3];
#pragma unroll
        for (int p = 0; p < 3; ++p) { const unsigned idx = 64 * p + lane, row = idx / 3, c = idx - row * 3; vi[p] = row * (DM * 2) + (c == 2 ? 20 : 16 * c) - p * 1024; }
#pragma unroll
        for (int g = 0; g < n; ++g) {
            const char* src = wq + nt * 36 + (g0 + g) * (64 * DM * 2);
            set_m0(lds0 + L_WQ + (g0 + g) * 3072);
            glds<0>(vi[0], src); glds<1024>(vi[1], src); glds<2048>(vi[2], src);
        }
    };
    u32x3 r[3];
    u64 tag;
    asm volatile("s_cmp_lt_u32 %8, 4\n\ts_cbranch_scc1 1f\n\t"
                 "global_load_dwordx3 %0, %4, %5\n\tglobal_load_dwordx3 %1, %4, %5 offset:384\n\tglobal_load_dwordx3 %2, %4, %5 offset:768\n\t"
                 "global_load_dwordx2 %3, %6, %7 sc1\n\ts_branch 2f\n"
                 "1:\n\ts_cmp_lg_u32 %8, 0\n\ts_cbranch_scc1 2f\n\ts_mov_b64 exec, 0x3fff\n\tds_write_b32 %9, %6\n\ts_mov_b64 exec, -1\n\ts_waitcnt lgkmcnt(0)\n"
                 "2:\n\ts_barrier"
                 : "=&v"(r[0]), "=&v"(r[1]), "=&v"(r[2]), "=&v"(tag)
                 : "v"((2 * (w & 3) + hi) * (DM * 2) + l31 * 12), "s"(x + (size_t)bid * 8 * DM * 2), "v"(WS_EPOCH), "s"(ws), "s"(w), "v"(cb + 4 * lane) : "memory", "scc");
    if (w < 4) {
        const char* wsrc = ((w >> 1) ? w3 : w1) + (size_t)(32 * (w & 1)) * (FF * 2);
        const char* wsrcA = wsrc + 128 * nt;
        const char* wsrcB = wsrc + 4096 + 160 * nt;
        const unsigned dstW = lds0 + (w >> 1) * W_PART + (w & 1) * 5120;
        unsigned voffW[9];
#pragma unroll
        for (int p = 0; p < 5; ++p) { const unsigned idx = 64 * p + lane, row = idx / 10; voffW[p] = row * (FF * 2) + ((idx - row * 10) & 7) * 16 - (p & 3) * 1024; }
        auto w_piece = [&](auto TT, auto PP) {
            constexpr int T = TT.v, p = PP.v;
            if constexpr ((p & 3) == 0) set_m0(dstW + (T % 3) * W_SLOT + p * 1024);
            glds<(p & 3) * 1024>(voffW[p], T < 9 ? wsrcA + (size_t)T * 64 * FF * 2 : wsrcB + (size_t)(T - 9) * 64 * FF * 2);
        };
        unsigned pf;
        {
            const unsigned idx = min(16 * w + lane, 63) % 54, row = idx / 3;
            const unsigned vo = (18 * r_ + row) * (DM * 2) + ((sp * 288) & ~127) + (idx - row * 3) * 128;
            asm volatile("s_mov_b64 exec, %3\n\tglobal_load_dword %0, %1, %2\n\ts_mov_b64 exec, -1" : "=v"(pf) : "v"(vo), "s"(wq), "s"(0xffffull) : "memory");
        }
        static_for<0, 5>([&](auto P) { w_piece(IC<0>{}, P); });
        q_groups(w, 1);
        cnt_wait<C_C, 2>(cb, 2);
        {
            const char* isrc = ((w >> 1) ? wv : wk) - 2048 + (w & 1) * (256 * 36);
            unsigned vi[3];
#pragma unroll
            for (int p = 0; p < 3; ++p) { const unsigned idx = 64 * p + lane, row = idx / 3, c = idx - row * 3; vi[p] = row * 36 + (c == 2 ? 20 : 16 * c) + 2048 - p * 1024; }
#pragma unroll
            for (int g = 0; g < 5; ++g) {
                set_m0(lds0 + L_KV + (w >> 1) * 27648 + ((w & 1) * 4 + g) * 3072);
                glds<0>(vi[0], isrc + g * 2304); glds<1024>(vi[1], isrc + g * 2304); glds<2048>(vi[2], isrc + g * 2304);
            }
        }
        const unsigned wb = lds0 + ((lane >> 4) & 1) * W_PART + (8 * hi + ((lane >> 2) & 3)) * 160 + (lane & 3) * 8;
        f32x16 acc[5][2];
        const unsigned wqb = lds0 + L_WQ + (8 * hi + ((lane >> 2) & 3)) * 48 + ((lane & 16) ? 40 : 8 * (lane & 3));
        waitn<0>();
        cnt_post<C_K>(cb, one);
        cnt_wait<C_S>(cb, 4);
        tag = uni64(*(volatile const LDS_AS u64*)(size_t)(cb + C_TAG));
        flag_wait(ws, WS_FX + (mt * 32 + 8 * w + (lane & 7)) * FS, tag);
        PIN(pf);
        PIN(pc0);
        cnt_post<C_L>(cb, one);
        const int tb0 = mt * 8 + 2 * w;
        const char* xb = ws + WS_XN + (size_t)tb0 * 36864;
        unsigned vo16b = vo16 + 36864;
        PIN(vo16b);
        bf16x8 xr[6][2], wf[5], hr[HR];
        auto x_load = [&](auto SS) {
            constexpr int ks = SS.v % 36;
            ld16<(ks & 3) * 1024>(xr[SS.v % 6][0], vo16, xb + (ks >> 2) * 4096);
            ld16<(ks & 3) * 1024>(xr[SS.v % 6][1], vo16b, xb + (ks >> 2) * 4096);
        };
        static_for<0, 6>([&](auto S) { x_load(S); });
        static_for<0, 5>([&](auto P) { w_piece(IC<1>{}, P); });
        const int tbg = 4 * mt2 + w;
        const char* hbA = ws + WS_H + (size_t)(tbg * 288 + 32 * sp) * 1024;
        const char* hbB = ws + WS_H + (size_t)(tbg * 288 + 128 + 40 * sp) * 1024;
        const char* ob = ws + WS_O + (size_t)(tbg * 36) * 1024;
        auto h_load = [&](auto JJ) {
            constexpr int J = JJ.v, r = J < 32 ? J : J < 72 ? J - 32 : J - 72;
            if constexpr (J == 80) ld16<0>(hr[J % HR], vo16, ob + (32 + sp) * 1024);
            else ld16<(r & 3) * 1024>(hr[J % HR], vo16, (J < 32 ? hbA : J < 72 ? hbB : ob + sp * 8192) + (r >> 2) * 4096);
        };
        const char* w2A = w2 + ((size_t)(512 * sp + 32 * w) * DM + nt2 * 144) * 2;
        const char* w2B = w2 + ((size_t)(2048 + 640 * sp + 32 * w) * DM + nt2 * 144) * 2;
        const char* woS = wo + ((size_t)(18 * (8 * sp + 2 * w)) * DM + nt2 * 144) * 2;
        const unsigned dst2 = lds0 + w * 9216;
        auto w2_piece = [&](auto TT, auto PP) {
            constexpr int T = TT.v, p = PP.v, off = (p & 3) * 1024;
            if constexpr (off == 0) set_m0(dst2 + g2_slot(T) + p * 1024);
            if constexpr (T < 9) glds<off>(voffW[p], T < 4 ? w2A + (size_t)T * 128 * DM * 2 : w2B + (size_t)(T - 4) * 128 * DM * 2);
            else if constexpr (p == 4) {
                __builtin_amdgcn_s_setprio(3);
                SBAR();
                unsigned vo = voffW[p] + (vo16 >> 9) * (2 * DM * 2);
                PIN(vo);
                SBAR();
                __builtin_amdgcn_s_setprio(GPRIO);
                glds<off>(vo, woS);
            }
            else glds<off>(voffW[p], woS + (p > 4 ? 2 * DM * 2 : 0));
        };
        const unsigned fh = WS_FH + ((tbg >> 1) * 32 + 8 * sp + (lane & 7)) * FS;
        u64 fs;
        unsigned pk_c = 0, pk_l = 0;
        cnt_wait<C_L>(cb, 4);
        __builtin_amdgcn_s_setprio(GPRIO);
        const f32x16 zero = {};
        auto epilogue = [&](auto NBB, int ks0) {
            __builtin_amdgcn_s_setprio(NBB.v == 4 ? EPA : 3);
#pragma unroll
            for (int m = 0; m < 2; ++m)
#pragma unroll
                for (int u = 0; u < NBB.v; ++u) {
                    const f32x16& c = acc[u + 5 - NBB.v][m];
                    unsigned a0, a1, a2, a3;
                    swiglu4(a0, a1, c[0], c[1], c[2], c[3], c[8], c[9], c[10], c[11]);
                    swiglu4(a2, a3, c[4], c[5], c[6], c[7], c[12], c[13], c[14], c[15]);
                    st16(u32x4{a0, a1, a2, a3}, rs, vo16, WS_H + ((tb0 + m) * 288 + ks0 + u) * 1024);
                    SBAR();
                }
            __builtin_amdgcn_s_setprio(GPRIO);
        };
        static_for<0, 72>([&](auto SS) {
            constexpr int s = SS.v, NB = 5, NBUF = 5, T = s >> 2, sl = s % 36, UPS = 4 * NB;
            auto w_read = [&](auto GG) {
                constexpr int G = GG.v, st = (s / 36) * 36 + G / NB, off = ((st >> 2) % 3) * W_SLOT + (st & 3) * 2560 + (G % NB - (s < 36)) * 32;
                if constexpr (s < 36 && G % NB == 0) wf[G % NBUF] = lds_tr2(wqb + st * 768, wqb + st * 768 + 192);
                else wf[G % NBUF] = lds_tr2(wb + off, wb + off + 640);
            };
            if constexpr (sl == 0) { static_for<0, 4>([&](auto G) { w_read(G); }); SBAR(); }
            static_for<0, NB>([&](auto UU) {
                constexpr int u = UU.v, U = (s & 3) * NB + u, G = sl * NB + u;
                if constexpr (u == 0 && s < 6) waitv<g1_cum(s, 0) + 2 * (5 - s) + 5>(xr[s][0], xr[s][1]);
                if constexpr (u == 0 && s >= 6) {
                    if constexpr (s == S_CA) {
                        asm volatile("s_waitcnt vmcnt(%3)" : "+v"(xr[s % 6][0]), "+v"(xr[s % 6][1]), "+v"(fs) : "n"(g1_cum(s, 0) - g1_upto_loads(s - 6)) : "memory");
                        flag_check(ws, fh, tag, fs);
                    } else waitv<g1_cum(s, 0) - g1_upto_loads(s - 6)>(xr[s % 6][0], xr[s % 6][1]);
                    if constexpr (s == S_FA) flag_put(ws, WS_FH + ((mt * 4 + w) * 32 + nt) * FS, tag);
                }
                if constexpr (T < 17 && U == 12) {
                    waitn<g1_cum(s, u) - (T >= 1 ? g1_upto_unit(T - 1, 6) : 0)>();
                    cnt_post<C_L>(cb, one);
                }
                if constexpr (T == 17 && U == 15) { waitn<g1_cum(s, u) - g1_upto_unit(16, 10)>(); cnt_post<C_L>(cb, one); }
                if constexpr (U == 2) cnt_check<C_C, 4 * (T + 1)>(cb, pk_c);
                if constexpr (T == 3 && U == 3) cnt_wait<C_F>(cb, 2);
                if constexpr (T == 16 && U == 2) cnt_wait<C_A>(cb, 4);
                if constexpr (T < 17 && T != 8 && U == UPS - 4) cnt_check<C_L, 4 * (T + 2)>(cb, pk_l);
                if constexpr (U == 1) pk_c = cnt_peek<C_C>(cb);
                if constexpr (T < 17 && U == (T == 8 ? UPS - 1 : UPS - 5)) pk_l = cnt_peek<C_L>(cb);
                if constexpr (G + 4 < 36 * NB) w_read(IC<G + 4>{});
                if constexpr (T == 7 && U == 1) {
                    __builtin_amdgcn_s_setprio(3);
                    unsigned ln = vo16; PIN(ln); ln >>= 4;
#pragma unroll
                    for (int p = 0; p < 5; ++p) { const unsigned idx = 64 * p + ln; voffW[p] += (idx % 10 >= 8) ? 128 : 0; PIN(voffW[p]); }
                    __builtin_amdgcn_s_setprio(GPRIO);
                }
                if constexpr (T == 16 && U == 1) {
                    __builtin_amdgcn_s_setprio(3);
                    unsigned ln = vo16; PIN(ln); ln >>= 4;
#pragma unroll
                    for (int p = 0; p < 9; ++p) {
                        const unsigned idx = 64 * p + ln, r = idx / 18;
                        voffW[p] = ((r & 16) + kperm(r & 15)) * (DM * 2) + (idx - r * 18) * 16 - (p & 3) * 1024;
                        PIN(voffW[p]);
                    }
                    __builtin_amdgcn_s_setprio(GPRIO);
                }
                SBAR();
                acc[u][0] = MFMA32(wf[G % NBUF], xr[s % 6][0], sl == 0 ? zero : acc[u][0]);
                SBAR();
                if constexpr (U == UPS - 1) cnt_post<C_C>(cb, one);
                if constexpr (g1_piece(s, u)) {
                    if constexpr (U == 13) {
                        const unsigned b = min((T - PFG) * 128 + 4 * r_ + w, 1295) * 1024;
                        set_m0(lds0 + L_EX);
                        asm volatile("global_load_lds_dword %0, %1" :: "v"(vo16), "s"(w2 + b + (b < 512 * DM * 2 ? 512 * sp : 1536 + 640 * sp) * (DM * 2)) : "memory");
                    } else if constexpr (T <= 15) w_piece(IC<T + 2>{}, IC<U - 2>{});
                    else w2_piece(IC<T - 16>{}, IC<U - 2>{});
                }
                SBAR();
                acc[u][1] = MFMA32(wf[G % NBUF], xr[s % 6][1], sl == 0 ? zero : acc[u][1]);
                SBAR();
                if constexpr (s == 35 && u == 1) {
                    constexpr float QS = 0.23570226039551584f * 1.4426950408889634f;
                    __builtin_amdgcn_s_setprio(3);
#pragma unroll
                    for (int m = 0; m < 2; ++m) {
                        const f32x16& c = acc[0][m];
                        const unsigned dq = lds0 + L_Q + (2 * w + m) * 1280;
                        *(LDS_AS u32x4*)(size_t)(dq + vo16) = u32x4{pack2(c[0] * QS, c[1] * QS), pack2(c[2] * QS, c[3] * QS), pack2(c[4] * QS, c[5] * QS), pack2(c[6] * QS, c[7] * QS)};
                        *(LDS_AS unsigned*)(size_t)(dq + 1024 + (vo16 >> 2)) = pack2(c[10] * QS, c[11] * QS);
                    }
                    asm volatile("s_waitcnt lgkmcnt(0)" ::: "memory");
                    cnt_post<C_Q>(cb + 4 * (w >> 1), one);
                    __builtin_amdgcn_s_setprio(GPRIO);
                    SBAR();
                }
            });
            if constexpr (s == S_SA) ldflag(fs, fh, ws);
            if constexpr (s < 66) x_load(IC<s + 6>{});
            else { h_load(IC<2 * (s - 66)>{}); h_load(IC<2 * (s - 66) + 1>{}); }
            SBAR();
            if constexpr (s == 35) {
                epilogue(IC<4>{}, 4 * nt);
                cnt_check<C_L, 40>(cb, pk_l);
            }
        });
        __builtin_amdgcn_s_setprio(3);
        unsigned ln = vo16; PIN(ln); ln >>= 4;
        unsigned trl = lds0 + (2 * ((ln >> 2) & 3) + (ln >> 5)) * 288 + ((ln >> 4) & 1) * 32 + (ln & 3) * 8, ra = trl + G_X;
        unsigned fo = WS_FO + (((tbg >> 3) * 32 + 8 * sp + (ln & 7)) * 4 + ((tbg >> 2) & 1) + 2 * (tbg & 1)) * FS;
        PIN(trl); PIN(ra); PIN(fo);
        cnt_wait<C_L>(cb, 76);
        epilogue(IC<5>{}, 128 + 5 * nt);
        static_for<12, HR>([&](auto J) { h_load(J); });
        f32x16 ac[5];
        bf16x8 vf[2][5];
#pragma unroll
        for (int n = 0; n < 5; ++n) vf[0][n] = lds_tr2(ra + n * 64, ra + n * 64 + 2304);
        static_for<0, 81>([&](auto JJ) {
            constexpr int J = JJ.v, T = J >> 3, j = J & 7;
            if constexpr (j == 5 && T < 9) {
                waitn<g2_cum(J) - (T == 0 ? g1_upto_unit(17, 10) : g2_cum(J - 8) + g2_pieces(J - 8))>();
                cnt_post<C_L>(cb, one);
            }
            if constexpr (j == 1 && T < 9) cnt_check<C_C, 4 * (19 + T)>(cb, pk_c);
            if constexpr (j == 7 && T < 9) {
                asm volatile("s_waitcnt lgkmcnt(0)" ::: "memory");
                if constexpr (T < 8) cnt_post<C_C>(cb, one);
                cnt_check<C_L, 4 * (20 + T)>(cb, pk_l);
                ra = add_now<g2_slot(T + 1)>(trl);
            }
            if constexpr (J == 79) ra = add_now<G_L>(trl);
            waitv<g2_cum(J) - g2_upto_frag(J)>(hr[J % HR]);
            if constexpr (j == 0 && T < 9) pk_c = cnt_peek<C_C>(cb);
            if constexpr (j == 6 && T < 9) pk_l = cnt_peek<C_L>(cb);
            SBAR();
            static_for<0, 5>([&](auto NN) {
                constexpr int n = NN.v, nx = ((J + 1) & 7) * 4608 + n * 64;
                if constexpr (J < 80) vf[(J + 1) & 1][n] = lds_tr2(ra + nx, ra + nx + 2304);
                SBAR();
                ac[n] = MFMA32(vf[J & 1][n], hr[J % HR], J == 0 ? zero : ac[n]);
                SBAR();
                if constexpr (J < 64 && j >= 1 && j <= 5 && (n == 1 || (n == 3 && j < 5))) w2_piece(IC<T + 2>{}, IC<2 * (j - 1) + n / 2>{});
                if constexpr (J == 6 && (n == 1 || n == 3)) {
                    __builtin_amdgcn_s_setprio(3);
                    SBAR();
                    unsigned ln = vo16; PIN(ln); ln >>= 4;
                    const unsigned pc = n == 1 ? w : 4, idx = min(64 * pc + ln, 287u), r = idx / 18, a_ = r >> 3, b_ = r & 1, c_ = (r >> 1) & 3;
                    unsigned vo = (18 * (4 * b_ + a_ + (c_ & 2)) + 16 + (c_ & 1)) * (DM * 2) + (idx - r * 18) * 16;
                    PIN(vo);
                    SBAR();
                    __builtin_amdgcn_s_setprio(GPRIO);
                    set_m0(lds0 + G_L + pc * 1024);
                    glds<0>(vo, wo + ((size_t)(144 * sp) * DM + nt2 * 144) * 2);
                }
                SBAR();
            });
            if constexpr (J == J_FB) {
                waitn<g2_cum(J) + g2_pieces(J) - (g1_cum(72, 0) + 10)>();
                flag_put(ws, WS_FH + ((32 + mt * 4 + w) * 32 + nt) * FS, tag);
            }
            if constexpr (J == J_SB) ldflag(fs, fh, ws + 1024 * FS);
            if constexpr (J == J_CB) { waitv<g2_cum(J) + g2_pieces(J) - (g2_cum(J_SB) + g2_pieces(J_SB) + 1)>(fs); flag_check(ws + 1024 * FS, fh, tag, fs); }
            if constexpr (J == J_SO) ldflag(fs, fo, ws);
            if constexpr (J == J_CO) {
                waitv<g2_cum(J) + g2_pieces(J) - (g2_cum(J_SO) + g2_pieces(J_SO) + 1)>(fs);
                flag_check(ws, fo, tag, fs);
            }
            if constexpr (g2_next(J) >= 0) h_load(IC<g2_next(J)>{});
            SBAR();
        });
        __builtin_amdgcn_s_setprio(3);
        if (w != sp) {
            const unsigned dst = ((bid & ~6) | (w << 1)) * 4 + sp;
#pragma unroll
            for (int g = 0; g < 9; ++g) {
                const f32x16& a = ac[g >> 1];
                const int o = 8 * (g & 1);
                auto r0 = __builtin_amdgcn_permlane32_swap(pack2(a[o], a[o + 1]), pack2(a[o + 4], a[o + 5]), false, false);
                auto r1 = __builtin_amdgcn_permlane32_swap(pack2(a[o + 2], a[o + 3]), pack2(a[o + 6], a[o + 7]), false, false);
                st16(u32x4{r0[0], r1[0], r0[1], r1[1]}, rs, vo16, WS_P + dst * 9216 + g * 1024);
            }
            waitn<0>();
            flag_set(ws, WS_FP + dst * FPS, tag, vo16);
        } else {
#pragma unroll
            for (int c = 0; c < 18; ++c) {
                const f32x16& a = ac[c >> 2];
                lds_st4(lds0 + G_Y + c * 1024 + (vo16 & 496) * 2 + (vo16 >> 9) * 16, a[4 * (c & 3)], a[4 * (c & 3) + 1], a[4 * (c & 3) + 2], a[4 * (c & 3) + 3]);
            }
            asm volatile("s_waitcnt lgkmcnt(0)" ::: "memory");
            cnt_post<C_P>(cb, one);
        }
    } else {
        const int a = w - 4, par = a & 1, kh = a >> 1, kh2 = a & 1;
        const bool own8 = (l31 >> 3) == a;
        __builtin_amdgcn_s_setprio(3);
        {
            asm volatile("s_waitcnt vmcnt(0)" : "+v"(r[0]), "+v"(r[1]), "+v"(r[2]), "+v"(tag) :: "memory");
            float v[18], s = 0.f, q = 0.f;
#pragma unroll
            for (int i = 0; i < 9; ++i) { v[2 * i] = blo(r[i / 3][i % 3]); v[2 * i + 1] = bhi(r[i / 3][i % 3]); s += v[2 * i] + v[2 * i + 1]; }
            const float mu = half_sum(s) * (1.f / DM), mub = bf16r(mu);
#pragma unroll
            for (int i = 0; i < 18; ++i) q = fmaf(v[i] - mu, v[i] - mu, q);
            const float rstd = __builtin_amdgcn_rcpf(bf16r(__builtin_amdgcn_sqrtf(bf16r(bf16r(half_sum(q) * (1.f / DM)) + 1e-5f))));
            const unsigned dst = lds0 + L_XS + (2 * a + hi) * 1168 + l31 * 12;
#pragma unroll
            for (int i = 0; i < 9; ++i) {
                const unsigned d = pack2(v[2 * i] - mub, v[2 * i + 1] - mub);
                *(LDS_AS unsigned*)(size_t)(dst + (i / 3) * 384 + (i % 3) * 4) = pack2(blo(d) * rstd, bhi(d) * rstd);
            }
            tag = uni64(tag) + TAG_STEP;
            if (a == 0 && lane == 0) *(LDS_AS u64*)(size_t)(cb + C_TAG) = tag;
            asm volatile("s_waitcnt lgkmcnt(0)" ::: "memory");
            cnt_post<C_S>(cb, one);
            cnt_wait<C_S, 0>(cb, 4);
            if (__builtin_expect(a >= 2, 1)) {
#pragma unroll
                for (int i = 0; i < 5; ++i) {
                    const u32x4 d = *(const LDS_AS u32x4*)(size_t)(lds0 + L_XS + (lane & 7) * 1168 + ((4 * kh2 + i) * 8 + (lane >> 3)) * 16);
                    st16(d, rs, (lane >> 3) * 512 + (lane & 7) * 16, WS_XN + (bid >> 2) * 36864 + (bid & 3) * 128 + (4 * kh2 + i) * 4096);
                }
                cnt_post<C_C>(cb, one);
                waitn<0>();
                if (cnt_add(cb + C_B) == 1) flag_set(ws, WS_FX + bid * FS, tag, lane);
                q_groups(4 + 2 * kh2, 3);
                waitn<0>();
                cnt_post<C_F>(cb, one);
            } else {
                const unsigned ba = lds0 + L_KV + a * 27648 + (8 * hi + ((lane >> 2) & 3)) * 48 + ((lane & 16) ? 40 : 8 * (lane & 3));
                const unsigned xa = lds0 + L_XS + (a ? (l31 & 3) | ((l31 >> 1) & 4) : l31 & 7) * 1168 + hi * 16;
                f32x16 c = {};
                bf16x8 fw[5], fx[5];
                auto kv_read = [&](auto I) { fw[I.v % 5] = lds_tr2(ba + I.v * 768, ba + I.v * 768 + 192); fx[I.v % 5] = *(const LDS_AS bf16x8*)(size_t)(xa + I.v * 32); };
                cnt_wait<C_K, 0>(cb, 4);
                static_for<0, 4>(kv_read);
                if (a == 0) {
                    static_for<0, 36>([&](auto I) {
                        if constexpr (I.v + 4 < 36) kv_read(IC<I.v + 4>{});
                        SBAR(); c = MFMA32(fw[I.v % 5], fx[I.v % 5], c); SBAR();
                    });
                    PIN(c);
                    cnt_post<C_C>(cb, one);
                    const unsigned p0 = pack2(c[0], c[1]), p1 = pack2(c[2], c[3]), p2 = pack2(c[4], c[5]), p3 = pack2(c[6], c[7]), p4 = pack2(c[10], c[11]);
                    st16(u32x4{p0, p1, p2, p3}, rs, l31 < 8 ? hi * 512 + l31 * 16 : 0x80000000u, WS_K1 + (bid >> 2) * 1024 + (bid & 3) * 128);
                    __builtin_amdgcn_raw_buffer_store_b64(u32x2{p4, 0x3f80u}, rs, lane < 8 ? lane * 8 : 0x80000000u, WS_K2 + (bid >> 2) * 256 + (bid & 3) * 64, SC1);
                    float n2 = hi ? 0.f : blo(p4) * blo(p4) + bhi(p4) * bhi(p4);
                    n2 += blo(p0) * blo(p0) + bhi(p0) * bhi(p0) + blo(p1) * blo(p1) + bhi(p1) * bhi(p1);
                    n2 += blo(p2) * blo(p2) + bhi(p2) * bhi(p2) + blo(p3) * blo(p3) + bhi(p3) * bhi(p3);
                    const float kmax = half_max(sum32(n2));
                    waitn<0>();
                    flag_set(ws, WS_FKV + bid * 16, (u64)f2u(kmax) << 32 | (unsigned)tag, lane);
                } else {
                    static_for<0, 36>([&](auto I) {
                        if constexpr (I.v + 4 < 36) kv_read(IC<I.v + 4>{});
                        SBAR(); c = MFMA32(fx[I.v % 5], fw[I.v % 5], c); SBAR();
                    });
                    PIN(c);
                    cnt_post<C_C>(cb, one);
                    const u32x4 mv = {pack2(c[0], c[1]), pack2(c[2], c[3]), pack2(c[4], c[5]), pack2(c[6], c[7])};
                    st16(mv, rs, (lane < 16 || (lane & 62) == 18) ? (lane < 16 ? lane : lane - 2) * 16 : 0x80000000u, WS_V + bid * 288);
                    waitn<0>();
                    flag_set(ws, WS_FKV + bid * 16 + 8, tag, lane);
                }
            }
        }
        const f32x16 zero = {};
        bf16x8 q1[4];
        unsigned ql[4];
        float km;
        const char* kb = ws + WS_K1 + kh * 32 * 1024;
        const char* k2b = ws + WS_K2 + kh * 32 * 256;
        const char* vb = ws + WS_V + kh * 32 * 1152;
        unsigned vo8 = ((l31 >> 4) * 2 + ((l31 >> 2) & 1)) * 64 + (((l31 >> 3) & 1) * 4 + (l31 & 3)) * 8, vok = vo8 * 2 + hi * 512;
        unsigned vov = hi * 288 + l31 * 16;
        PIN(vo8); PIN(vok); PIN(vov);
        bf16x8 k1[4], v0[4], v1[4];
        s16x4 k2[4], q2[4];
#pragma unroll
        for (int i = 0; i < 4; ++i) { const unsigned cs = l31 == 18 ? 0x3f803f80u : 0u; v0[i] = v1[i] = __builtin_bit_cast(bf16x8, u32x4{cs, cs, cs, cs}); PIN(v0[i]); PIN(v1[i]); }
        {
            const char* fkv = ws + WS_FKV + kh * 2048;
            bf16x8 f0, f1;
            unsigned pf = 0;
            __builtin_amdgcn_s_setprio(0);
            const unsigned vta = (2 * r_ + (a & 1)) * (FF * 2) + 1024 * sp + min(lane, 7) * 128;
            const unsigned vtb = (2 * r_ + (a & 1)) * (FF * 2) + 4096 + 1280 * sp + min(lane, 9) * 128;
            auto side = [&](int i) {
                cnt_wait<C_L, 8>(cb, 4 * (i + 1));
                if (i < 5) touch(pf, vta, ((a >> 1) ? w3 : w1) + (size_t)(i + 4) * 64 * FF * 2);
                if (i >= 4) touch(pf, vtb, ((a >> 1) ? w3 : w1) + (size_t)(i - 4) * 64 * FF * 2);
            };
#pragma unroll
            for (int i = 0; i < KVW / 4 - 1; ++i) side(i);
            cnt_wait<C_L, 8>(cb, KVW);
            __builtin_amdgcn_s_setprio(3);
            ld16<0>(f0, vo16, fkv); ld16<1024>(f1, vo16, fkv);
            for (unsigned lim = SPIN_MAX; lim; --lim) {
                waitv<0>(f0, f1);
                const u32x4 a0 = __builtin_bit_cast(u32x4, f0), a1 = __builtin_bit_cast(u32x4, f1);
                const unsigned tl = (unsigned)tag, tu = (unsigned)(tag >> 32);
                if (all_lanes(a0[0] == tl && a0[2] == tl && a0[3] == tu && a1[0] == tl && a1[2] == tl && a1[3] == tu)) break;
                __builtin_amdgcn_s_sleep(16);
                ld16<0>(f0, vo16, fkv); ld16<1024>(f1, vo16, fkv);
            }
            waitv<0>(f0, f1);
            km = fmaxf(u2f(__builtin_bit_cast(u32x4, f0)[1]), u2f(__builtin_bit_cast(u32x4, f1)[1]));
            ld16<0>(k1[0], vok, kb); ld8<0>(k2[0], vo8, k2b);
            static_for<0, 3>([&](auto B) {
                ld16<(B.v + 1) * 1024>(k1[B.v + 1], vok, kb); ld8<(B.v + 1) * 256>(k2[B.v + 1], vo8, k2b);
                ldv(v0[B.v], v1[B.v], vov, vb + B.v * 1152);
            });
            km = max32(half_max(km));
            PIN(km);
            __builtin_amdgcn_s_setprio(0);
#pragma unroll
            for (int i = KVW / 4 - 1; i < 9; ++i) side(i);
            const unsigned l17 = min(lane, 17);
            touch(pf, l17 < 9 ? (4 * r_ + a) * (DM * 2) + l17 * 128 : (128 + ((4 * r_ + a) & 15)) * (DM * 2) + (l17 - 9) * 128, wo + (size_t)144 * sp * (DM * 2));
            cnt_wait<C_L, 8>(cb, 40);
            __builtin_amdgcn_s_setprio(3);
            cnt_wait<C_Q, 1>(cb + 4 * par, 2);
#pragma unroll
            for (int qb = 0; qb < 4; ++qb) {
                const unsigned src = lds0 + L_Q + (4 * par + qb) * 1280;
                q1[qb] = *(const LDS_AS bf16x8*)(size_t)(src + vo16);
                ql[qb] = *(const LDS_AS unsigned*)(size_t)(src + 1024 + l31 * 4);
            }
            asm volatile("s_waitcnt lgkmcnt(0)" ::: "memory");
#pragma unroll
            for (int qb = 0; qb < 4; ++qb) { PIN(q1[qb]); PIN(ql[qb]); }
            cnt_post<C_A>(cb, one);
            waitv<0>(pf);
        }
        float mrow[4];
        bool safe = true;
#pragma unroll
        for (int qb = 0; qb < 4; ++qb) {
            const u32x4 u = __builtin_bit_cast(u32x4, q1[qb]);
            float n2 = hi ? 0.f : blo(ql[qb]) * blo(ql[qb]) + bhi(ql[qb]) * bhi(ql[qb]);
#pragma unroll
            for (int i = 0; i < 4; ++i) n2 += blo(u[i]) * blo(u[i]) + bhi(u[i]) * bhi(u[i]);
            const float bound = bf16r(__builtin_amdgcn_sqrtf(sum32(n2) * km) * 1.01f);
            safe = safe && (bound <= SM_SAFE);
            mrow[qb] = bound;
            q2[qb] = __builtin_bit_cast(s16x4, u32x2{hi ? 0u : ql[qb], hi ? 0u : pack2(-bound, 0.f)});
        }
        f32x16 o[4];
#pragma unroll
        for (int qb = 0; qb < 4; ++qb) o[qb] = f32x16{};
        asm volatile("s_waitcnt vmcnt(0)" : "+v"(k1[0]), "+v"(k1[1]), "+v"(k1[2]), "+v"(k1[3]), "+v"(k2[0]), "+v"(k2[1]), "+v"(k2[2]), "+v"(k2[3]),
                     "+v"(v0[0]), "+v"(v0[1]), "+v"(v0[2]), "+v"(v1[0]), "+v"(v1[1]), "+v"(v1[2]) :: "memory");
        __builtin_amdgcn_s_setprio(LOWT ? 0 : APRIO);
        if (all_lanes(safe)) {
            f32x16 s[2];
            unsigned p0[4], p1[4] = {0u, 0u, 0u, 0u};
            s[0] = MFMA32(k1[0], q1[0], zero); s[0] = MFMA8(k2[0], q2[0], s[0]);
            SBAR();
            for (int t4 = 0; t4 < 32; t4 += 4) {
                static_for<0, 16>([&](auto UU) {
                    constexpr int b = UU.v >> 2, qb = UU.v & 3, bn = (qb == 3) ? (b + 1) & 3 : b, bp = (qb == 0) ? (b + 3) & 3 : b;
                    f32x16& c = s[qb & 1];
                    f32x16& nx = s[(qb + 1) & 1];
                    float e0, e1, e2, e3;
#define EXP2(a, b, i) a = __builtin_amdgcn_exp2f(c[i]); b = __builtin_amdgcn_exp2f(c[i + 1]); PIN(a); PIN(b)
#define CVT2(p, i) p[i] = pack2(e0, e1); p[i + 1] = pack2(e2, e3); PIN(p[i]); PIN(p[i + 1])
                    if constexpr (LOWT && qb == 0 && b == (LOWT & 3)) { if (t4 == (LOWT & ~3)) __builtin_amdgcn_s_setprio(APRIO); }
                    EXP2(e0, e1, 0);
                    if constexpr (qb == 3) waitv<10>(k1[bn], k2[bn]);
                    SBAR();
                    nx = MFMA32(k1[bn], q1[(qb + 1) & 3], zero);
                    SBAR();
                    EXP2(e2, e3, 2); CVT2(p0, 0); EXP2(e0, e1, 4);
                    SBAR();
                    nx = MFMA8(k2[bn], q2[(qb + 1) & 3], nx);
                    SBAR();
                    if constexpr (qb == 3) {
                        const int tn = min(t4 + b + 4, 31);
                        ld16<0>(k1[b], vok, kb + tn * 1024); ld8<0>(k2[b], vo8, k2b + tn * 256);
                    }
                    EXP2(e2, e3, 6); CVT2(p0, 2); EXP2(e0, e1, 8);
                    SBAR();
                    o[(qb + 3) & 3] = MFMA32(v1[bp], __builtin_bit_cast(bf16x8, u32x4{p1[0], p1[1], p1[2], p1[3]}), o[(qb + 3) & 3]);
                    SBAR();
                    if constexpr (qb == 1) {
                        ldv(v0[(b + 3) & 3], v1[(b + 3) & 3], vov, vb + min(t4 + b + 3, 31) * 1152);
                    }
                    EXP2(e2, e3, 10); CVT2(p1, 0); EXP2(e0, e1, 12);
                    if constexpr (qb == 0) waitv<10>(v0[b], v1[b]);
                    SBAR();
                    o[qb] = MFMA32(v0[b], __builtin_bit_cast(bf16x8, u32x4{p0[0], p0[1], p0[2], p0[3]}), o[qb]);
                    SBAR();
                    EXP2(e2, e3, 14); CVT2(p1, 2);
                    SBAR();
                });
            }
            o[3] = MFMA32(v1[3], __builtin_bit_cast(bf16x8, u32x4{p1[0], p1[1], p1[2], p1[3]}), o[3]);
            asm volatile("s_waitcnt vmcnt(0)" : "+v"(k1[0]), "+v"(k1[1]), "+v"(k1[2]), "+v"(k1[3]), "+v"(k2[0]), "+v"(k2[1]), "+v"(k2[2]), "+v"(k2[3]),
                         "+v"(v0[0]), "+v"(v0[1]), "+v"(v0[2]), "+v"(v1[0]), "+v"(v1[1]), "+v"(v1[2]) :: "memory");
        } else {
#pragma unroll
            for (int qb = 0; qb < 4; ++qb) { q2[qb][2] = 0; mrow[qb] = -3.0e38f; }
#pragma unroll 1
            for (int t = 0; t < 32; ++t) {
                ld16<0>(k1[0], vok, kb + t * 1024); ld8<0>(k2[0], vo8, k2b + t * 256);
                ldv(v0[0], v1[0], vov, vb + t * 1152);
                waitv<0>(k1[0], k2[0]); waitv<0>(v0[0], v1[0]);
#pragma unroll
                for (int qb = 0; qb < 4; ++qb) {
                    f32x16 c = MFMA32(k1[0], q1[qb], zero);
                    c = MFMA8(k2[0], q2[qb], c);
                    float mx = c[0];
#pragma unroll
                    for (int r = 1; r < 16; ++r) mx = fmaxf(mx, c[r]);
                    const float mn = fmaxf(mrow[qb], max32(mx)), al = __builtin_amdgcn_exp2f(mrow[qb] - mn);
                    mrow[qb] = mn;
                    u32x4 p0, p1;
#pragma unroll
                    for (int r = 0; r < 4; ++r) {
                        p0[r] = pack2(__builtin_amdgcn_exp2f(c[2 * r] - mn), __builtin_amdgcn_exp2f(c[2 * r + 1] - mn));
                        p1[r] = pack2(__builtin_amdgcn_exp2f(c[8 + 2 * r] - mn), __builtin_amdgcn_exp2f(c[9 + 2 * r] - mn));
                    }
#pragma unroll
                    for (int r = 0; r < 16; ++r) o[qb][r] *= al;
                    o[qb] = MFMA32(v0[0], __builtin_bit_cast(bf16x8, p0), o[qb]);
                    o[qb] = MFMA32(v1[0], __builtin_bit_cast(bf16x8, p1), o[qb]);
                }
            }
        }
        __builtin_amdgcn_s_setprio(MPRIO);
        const unsigned ex = lds0 + L_EX + vo16;
#pragma unroll
        for (int j = 0; j < 2; ++j) {
            const f32x16& s = kh ? o[2 * j] : o[2 * j + 1];
            const float mm = kh ? mrow[2 * j] : mrow[2 * j + 1];
            lds_st4(ex + a * 6144 + j * 3072, s[0], s[1], s[2], s[3]);
            lds_st4(ex + a * 6144 + j * 3072 + 1024, s[4], s[5], s[6], s[7]);
            lds_st4(ex + a * 6144 + j * 3072 + 2048, s[8], s[9], s[10], mm);
        }
        asm volatile("s_waitcnt lgkmcnt(0)" ::: "memory");
        cnt_post<C_X>(cb + 4 * par, one);
        cnt_wait<C_X, 0>(cb + 4 * par, 2);
#pragma unroll
        for (int j = 0; j < 2; ++j) {
            const f32x16& s = kh ? o[2 * j + 1] : o[2 * j];
            const float ma = kh ? mrow[2 * j + 1] : mrow[2 * j];
            const unsigned src = ex + (a ^ 2) * 6144 + j * 3072;
            const f32x4 t0 = lds_f4(src), t1 = lds_f4(src + 1024), t2 = lds_f4(src + 2048);
            const float mb = t2[3], mm = fmaxf(ma, mb);
            const float fa = __builtin_amdgcn_exp2f(ma - mm), fb = __builtin_amdgcn_exp2f(mb - mm);
            const float inv = __builtin_amdgcn_rcpf(low32(s[10] * fa + t2[2] * fb)), ia = fa * inv, ib = fb * inv;
            const u32x4 pk = {pack2(s[0] * ia + t0[0] * ib, s[1] * ia + t0[1] * ib), pack2(s[2] * ia + t0[2] * ib, s[3] * ia + t0[3] * ib),
                              pack2(s[4] * ia + t1[0] * ib, s[5] * ia + t1[1] * ib), pack2(s[6] * ia + t1[2] * ib, s[7] * ia + t1[3] * ib)};
            const int tb = 8 * mt + 4 * par + 2 * j + kh;
            st16(pk, rs, vo16, WS_O + (tb * 36 + nt) * 1024);
            __builtin_amdgcn_raw_buffer_store_b32(pack2(s[8] * ia + t2[0] * ib, s[9] * ia + t2[1] * ib), rs, hi ? 0x80000000u : vo16,
                                                  WS_O + (tb * 36 + 32 + sp) * 1024 + (nt & 4) * 128 + (nt & 1) * 8 + (nt & 2) * 2, SC1);
        }
        waitn<0>();
        flag_set(ws, WS_FO + ((mt * 32 + nt) * 4 + a) * FS, tag, lane);
        __builtin_amdgcn_s_setprio(0);
        const size_t orow = (size_t)((4 * mt2 + sp) * 32 + l31) * (DM * 2) + nt2 * 288 + hi * 16;
        u32x4 xr[3];
#pragma unroll
        for (int i = 0; i < 3; ++i) xr[i] = *(const u32x4*)(x + orow + (i < 2 ? a + 4 * i : 8) * 32);
        if (bid == 0 && a == 1) {
#pragma unroll
            for (int r = 0; r < 4; ++r) flag_wait(ws, WS_FX + (r * 64 + lane) * FS, tag);
            flag_set(ws, WS_EPOCH, tag - TAG_STEP + 1, lane);
        }
        cnt_wait<C_P, 4>(cb, 1);
        f32x4 s0[3], s1[3];
#pragma unroll
        for (int i = 0; i < 3; ++i) {
            const unsigned src = lds0 + G_Y + (i < 2 ? a + 4 * i : 8) * 2048 + lane * 32;
            s0[i] = lds_f4(src) + f32x4{blo(xr[i][0]), bhi(xr[i][0]), blo(xr[i][1]), bhi(xr[i][1])};
            s1[i] = lds_f4(src + 16) + f32x4{blo(xr[i][2]), bhi(xr[i][2]), blo(xr[i][3]), bhi(xr[i][3])};
            PIN(s0[i]); PIN(s1[i]);
        }
        flag_wait(ws, WS_FP + (bid * 4 + ((sp + 1 + min(lane, 2)) & 3)) * FPS, tag);
        u32x4 pp[3][3];
#pragma unroll
        for (int r = 0; r < 3; ++r)
#pragma unroll
            for (int i = 0; i < 3; ++i)
                pp[r][i] = __builtin_amdgcn_raw_buffer_load_b128(rs, (i < 2 || own8) ? vo16 : 0x80000000u,
                                                                 WS_P + (bid * 4 + ((sp + 1 + r) & 3)) * 9216 + (i < 2 ? a + 4 * i : 8) * 1024, SC1);
        SBAR();
#pragma unroll
        for (int i = 0; i < 3; ++i) {
#pragma unroll
            for (int r = 0; r < 3; ++r) {
                s0[i] += f32x4{blo(pp[r][i][0]), bhi(pp[r][i][0]), blo(pp[r][i][1]), bhi(pp[r][i][1])};
                s1[i] += f32x4{blo(pp[r][i][2]), bhi(pp[r][i][2]), blo(pp[r][i][3]), bhi(pp[r][i][3])};
            }
            if (i < 2 || own8)
                *(u32x4*)(out + orow + (i < 2 ? a + 4 * i : 8) * 32) = u32x4{pack2(s0[i][0], s0[i][1]), pack2(s0[i][2], s0[i][3]), pack2(s1[i][0], s1[i][1]), pack2(s1[i][2], s1[i][3])};
        }
    }
}

#ifdef __HIP_DEVICE_COMPILE__
#pragma clang attribute pop
#endif

extern "C" void kernel_launch(void* const* d_in, const int* in_sizes, int n_in, void* d_out, int out_size, void* d_ws, size_t ws_size,
                              hipStream_t stream) {
    (void)hipFuncSetAttribute((const void*)k_all, hipFuncAttributeMaxDynamicSharedMemorySize, LDS_BYTES);
    k_all<<<256, 512, LDS_BYTES, stream>>>((const char*)d_in[0], (const char*)d_in[1], (const char*)d_in[2], (const char*)d_in[3],
                                           (const char*)d_in[4], (const char*)d_in[6], (const char*)d_in[7], (const char*)d_in[8],
                                           (char*)d_ws, (char*)d_out);
}
```
